```python
import torch

def _softmax(x, axis=-1):
    return torch.softmax(x, dim=axis)
def _gelu(x):
    return torch.nn.functional.gelu(x, approximate="tanh")
def _silu(x):
    return torch.nn.functional.silu(x)
def _sigmoid(x):
    return torch.sigmoid(x)
def _layernorm(x, axis=-1, eps=1e-5):
    m = x.mean(dim=axis, keepdim=True)
    v = x.var(dim=axis, keepdim=True, unbiased=False)
    return (x - m) / torch.sqrt(v + eps)
def _scatter_add(tgt, idx, src, axis=0):
    return tgt.clone().index_add_(axis, idx, src)
def _segment_sum(src, seg, n):
    out = torch.zeros((n,) + src.shape[1:], dtype=src.dtype)
    return out.index_add_(0, seg, src)
def _pad(x, pw):
    flat = tuple(v for pair in reversed(pw) for v in pair)
    return torch.nn.functional.pad(x, flat)

kernel_name = 'sequential_preLN__spectral_gfnet__dense_activation_silu__prompt'

def setup_inputs():

    _w = (lambda a: (lambda t: t.to(torch.bfloat16) if t.is_floating_point() else t)(torch.as_tensor(a)))
    def _A_setup():
        B, S, D = 1, 2048, 512
        Df = D // 2 + 1
        return {
            "x": _w((lambda *s: torch.randn(*s, dtype=torch.bfloat16) * 0.05)(B, S, D)),
            "w_r": _w((lambda *s: torch.randn(*s, dtype=torch.bfloat16) * 0.4)(S, Df) * 0.02),
            "w_i": _w((lambda *s: torch.randn(*s, dtype=torch.bfloat16) * 0.4)(S, Df) * 0.02),
        }
    _A_in = _A_setup()

    def _B_setup():
        B, S, D, F = 1, 2048, 512, 1792
        return {
            "x": _w((lambda *s: torch.randn(*s, dtype=torch.bfloat16) * 0.05)(B, S, D)),
            "w1": _w((lambda *s: torch.randn(*s, dtype=torch.bfloat16) * 0.8)(D, F) * (D ** -0.5)),
            "w2": _w((lambda *s: torch.randn(*s, dtype=torch.bfloat16) * 0.1)(F, D) * (F ** -0.5)),
        }
    _B_in = _B_setup()

    return {
        "A_x": _A_in["x"],
        "A_w_r": _A_in["w_r"],
        "A_w_i": _A_in["w_i"],
        "B_x": _B_in["x"],
        "B_w1": _B_in["w1"],
        "B_w2": _B_in["w2"],
    }

def reference(A_x, A_w_r, A_w_i, B_x, B_w1, B_w2):
    def _A_ref(x, w_r, w_i):
        B, S, D = x.shape
        X = (lambda x, axes=(-2,-1): torch.fft.rfft2(x.float(), dim=axes))(x, axes=(-2, -1))
        W = w_r + 1j * w_i
        X = X * W
        y = (lambda x, s, axes=(-2,-1): torch.fft.irfft2(x, s=s, dim=axes).to(torch.bfloat16))(X, (S, D), axes=(-2, -1))
        return y

    def _B_ref(x, w1, w2):
        h = torch.einsum("bsd,df->bsf", x, w1)
        h = _silu(h)
        out = torch.einsum("bsf,fd->bsd", h, w2)
        return out

    x = A_x
    h = x + _A_ref(_layernorm(x, axis=-1), A_w_r, A_w_i)
    h2 = h + _B_ref(_layernorm(h, axis=-1), B_w1, B_w2)
    return h2
```

```cpp
#include <hip/hip_runtime.h>
#include <hip/hip_bf16.h>

typedef __bf16 bf16x8 __attribute__((ext_vector_type(8)));
typedef __bf16 bf16x2 __attribute__((ext_vector_type(2)));
typedef float f32x2 __attribute__((ext_vector_type(2)));
typedef float f32x4 __attribute__((ext_vector_type(4)));
typedef short s16x4 __attribute__((ext_vector_type(4)));
typedef unsigned u32x2 __attribute__((ext_vector_type(2)));
typedef unsigned u32x2u __attribute__((ext_vector_type(2), aligned(2)));
typedef unsigned u32x4 __attribute__((ext_vector_type(4)));
typedef unsigned short u16;
#define DI __device__ __forceinline__
#define LDSP __attribute__((address_space(3)))

constexpr int S = 2048, D = 512, DF = 257, F = 1792;
constexpr size_t COLB = 8192;
constexpr size_t OFF_Z = 0;
constexpr size_t OFF_W = OFF_Z + 260 * COLB;
constexpr size_t OFF_Y = OFF_W + 260 * COLB;
constexpr size_t OFF_A1 = OFF_Y + 260 * COLB;
constexpr size_t OFF_W1 = OFF_A1 + 2097152;
constexpr size_t OFF_W2 = OFF_W1 + 1835008;
constexpr size_t OFF_H = OFF_W2 + 1835008;

DI unsigned pk(float lo, float hi) {
  f32x2 f = {lo, hi};
  return __builtin_bit_cast(unsigned, __builtin_convertvector(f, bf16x2));
}
DI float bflo(unsigned v) { return __builtin_bit_cast(float, v << 16); }
DI float bfhi(unsigned v) { return __builtin_bit_cast(float, v & 0xffff0000u); }
DI float rbf(float x) { return bflo(pk(x, x)); }
DI f32x4 mfma(u32x4 a, u32x4 b, f32x4 c) {
  return __builtin_amdgcn_mfma_f32_16x16x32_bf16(__builtin_bit_cast(bf16x8, a), __builtin_bit_cast(bf16x8, b), c, 0, 0, 0);
}
DI float cosr(float r) { return __builtin_amdgcn_cosf(r); }
DI float sinr(float r) { return __builtin_amdgcn_sinf(r); }
template <int CTRL, int RM>
DI float dpp_add(float v) {
  return v + __builtin_bit_cast(float, __builtin_amdgcn_update_dpp(0, __builtin_bit_cast(int, v), CTRL, RM, 0xf, true));
}
DI float wave_sum(float v) {
  v = dpp_add<0xB1, 0xf>(v);
  v = dpp_add<0x4E, 0xf>(v);
  v = dpp_add<0x141, 0xf>(v);
  v = dpp_add<0x140, 0xf>(v);
  const int iv = __builtin_bit_cast(int, v);
  const float a = __builtin_bit_cast(float, __builtin_amdgcn_readlane(iv, 0));
  const float b = __builtin_bit_cast(float, __builtin_amdgcn_readlane(iv, 16));
  const float c = __builtin_bit_cast(float, __builtin_amdgcn_readlane(iv, 32));
  const float d = __builtin_bit_cast(float, __builtin_amdgcn_readlane(iv, 48));
  return (a + b) + (c + d);
}
DI void layernorm8(const float (&v)[8], float (&o)[8]) {
  float s = ((v[0] + v[1]) + (v[2] + v[3])) + ((v[4] + v[5]) + (v[6] + v[7]));
  const float mean = wave_sum(s) * (1.f / 512.f);
  float q = 0.f;
#pragma unroll
  for (int e = 0; e < 8; ++e) { const float d = v[e] - mean; q += d * d; }
  const float var = wave_sum(q) * (1.f / 512.f);
  const float mb = rbf(mean);
  const float sd = rbf(__builtin_amdgcn_sqrtf(rbf(rbf(var) + 1e-5f)));
  const float rs = __builtin_amdgcn_rcpf(sd);
#pragma unroll
  for (int e = 0; e < 8; ++e) o[e] = rbf(v[e] - mb) * rs;
}
DI int kcol(int k1, int k2) {
  if (k1 == 0) return k2 < 8 ? 32 * k2 : 32 * k2 - 240;
  return k2 < 8 ? k1 + 32 * k2 : 512 - k1 - 32 * k2;
}

__global__ __launch_bounds__(256) void k1_ln_rfft(const u16* __restrict__ x, const u16* __restrict__ wr,
                                                  const u16* __restrict__ wi, char* __restrict__ ws) {
  __shared__ __attribute__((aligned(16))) char lds[8192];
  const int lane = threadIdx.x & 63, wave = __builtin_amdgcn_readfirstlane(threadIdx.x >> 6);
  const int xcd = blockIdx.x & 7, bi = blockIdx.x >> 3;
  const int j = lane & 15, g = lane >> 4;
  if (wave < 2) {
    const int P = 2 * xcd + (bi >> 4), b0 = 2 * (bi & 15) + wave;
    u32x4 xv[4];
#pragma unroll
    for (int r = 0; r < 4; ++r) xv[r] = *(const u32x4*)(x + (size_t)(128 * P + 32 * r + b0) * D + 8 * lane);
    u32x4 BR, BI, AR, AI, SR, SI;
    float tc[4], ts[4];
#pragma unroll
    for (int h = 0; h < 4; ++h) {
      float c[2], s[2];
#pragma unroll
      for (int q = 0; q < 2; ++q) {
        const int e = 2 * h + q, a = 4 * g + (e & 3) + 16 * (e >> 2);
        const float ang = (float)((a * j) & 31) * (1.f / 32.f);
        c[q] = cosr(ang);
        s[q] = j == 0 ? ((a & 1) ? -1.f : 1.f) : -sinr(ang);
      }
      BR[h] = pk(c[0], c[1]);
      BI[h] = pk(s[0], s[1]);
      const int b = 4 * g + h;
      const float th = (float)((b * j) & 15) * (1.f / 16.f);
      const float ct = cosr(th), st = sinr(th);
      const float ph = (float)((b * (2 * j - 15)) & 31) * (1.f / 32.f);
      const float cp = cosr(ph), sp = sinr(ph);
      AR[h] = pk(ct, st);
      AI[h] = j < 8 ? pk(-st, ct) : pk(st, -ct);
      SR[h] = j < 8 ? pk(ct, 0.f) : pk(0.f, cp);
      SI[h] = j < 8 ? pk(j == 0 ? ((b & 1) ? -1.f : 1.f) : -st, 0.f) : pk(0.f, -sp);
      const float tw = (float)(b * j) * (1.f / 512.f);
      tc[h] = cosr(tw);
      ts[h] = sinr(tw);
    }
    char* wl = lds + wave * 4096;
#pragma unroll
    for (int r = 0; r < 4; ++r) {
      float v[8], o[8];
#pragma unroll
      for (int h = 0; h < 4; ++h) { v[2 * h] = bflo(xv[r][h]); v[2 * h + 1] = bfhi(xv[r][h]); }
      layernorm8(v, o);
      u32x4 u;
#pragma unroll
      for (int h = 0; h < 4; ++h) u[h] = pk(o[2 * h], o[2 * h + 1]);
      *(u32x4*)(wl + r * 1024 + lane * 16) = u;
    }
    u32x4 outv[4];
    unsigned nyq[4];
#pragma unroll
    for (int r = 0; r < 4; ++r) {
      const s16x4 t0 = __builtin_amdgcn_ds_read_tr16_b64_v4i16((LDSP s16x4*)(wl + r * 1024 + lane * 8));
      const s16x4 t1 = __builtin_amdgcn_ds_read_tr16_b64_v4i16((LDSP s16x4*)(wl + r * 1024 + 512 + lane * 8));
      const u32x2 a0 = __builtin_bit_cast(u32x2, t0), a1 = __builtin_bit_cast(u32x2, t1);
      const u32x4 a = {a0[0], a0[1], a1[0], a1[1]};
      const f32x4 z = {0.f, 0.f, 0.f, 0.f};
      const f32x4 dr = mfma(a, BR, z), di = mfma(a, BI, z);
      u32x4 t;
#pragma unroll
      for (int h = 0; h < 4; ++h) t[h] = pk(dr[h] * tc[h] + di[h] * ts[h], di[h] * tc[h] - dr[h] * ts[h]);
      const f32x4 xr = mfma(AR, t, z), xi = mfma(AI, t, z), sr = mfma(SR, t, z), si = mfma(SI, t, z);
#pragma unroll
      for (int h = 0; h < 4; ++h) outv[h][r] = j == 0 ? pk(sr[h], si[h]) : pk(xr[h], xi[h]);
      nyq[r] = outv[0][r] >> 16;
      if (lane == 0) outv[0][r] &= 0xffffu;
    }
    char* zb = ws + OFF_Z + (size_t)(P * 32 + b0) * 16;
#pragma unroll
    for (int h = 0; h < 4; ++h) *(u32x4*)(zb + (size_t)kcol(j, 4 * g + h) * COLB) = outv[h];
    if (lane == 0) *(u32x4*)(zb + (size_t)256 * COLB) = (u32x4){nyq[0], nyq[1], nyq[2], nyq[3]};
  } else {
    const int c = 64 * xcd + 2 * bi + (wave - 2);
    const int p0 = (c & 63) + 256 * (c >> 6);
    u32x2 a[4], b[4];
    u16 ea[4], eb[4];
#pragma unroll
    for (int r = 0; r < 4; ++r) {
      const size_t ro = (size_t)(p0 + 64 * r) * DF;
      a[r] = *(const u32x2u*)(wr + ro + 4 * lane);
      b[r] = *(const u32x2u*)(wi + ro + 4 * lane);
      ea[r] = wr[ro + 256];
      eb[r] = wi[ro + 256];
    }
    char* wb = ws + OFF_W + (size_t)c * 16;
#pragma unroll
    for (int q = 0; q < 4; ++q) {
      u32x4 o;
#pragma unroll
      for (int r = 0; r < 4; ++r)
        o[r] = __builtin_amdgcn_perm(b[r][q >> 1], a[r][q >> 1], (q & 1) ? 0x07060302u : 0x05040100u);
      *(u32x4*)(wb + (size_t)(4 * lane + q) * COLB) = o;
    }
    if (lane == 0) {
      u32x4 o;
#pragma unroll
      for (int r = 0; r < 4; ++r) o[r] = (unsigned)ea[r] | ((unsigned)eb[r] << 16);
      *(u32x4*)(wb + (size_t)256 * COLB) = o;
    }
  }
}

DI void retile_weights(const u16* __restrict__ w1, const u16* __restrict__ w2, char* __restrict__ ws, int wg, int wave, int lane) {
  const int n = lane & 15, g = lane >> 4;
  if (wg < 128) {
    const int cb = wg >> 3, ks0 = 2 * (wg & 7);
    for (int f = wave; f < 14; f += 4) {
      const int ks = ks0 + (f >= 7), t = f >= 7 ? f - 7 : f;
      const int o = 4 * ks + g, n0 = 16 * (o & 15) + 4 * (o >> 4);
      const u16* src = w1 + (size_t)n0 * F + 112 * cb + 16 * t + n;
      u16 v[8];
#pragma unroll
      for (int e = 0; e < 8; ++e) v[e] = src[(size_t)((e & 3) + 256 * (e >> 2)) * F];
      u32x4 o4;
#pragma unroll
      for (int h = 0; h < 4; ++h) o4[h] = (unsigned)v[2 * h] | ((unsigned)v[2 * h + 1] << 16);
      *(u32x4*)(ws + OFF_W1 + ((size_t)((cb * 16 + ks) * 7 + t) * 64 + lane) * 16) = o4;
    }
  } else {
    const int f0 = (wg - 128) * 14;
    for (int f = wave; f < 14; f += 4) {
      const int fr = f0 + f, dt = fr & 3, ks = (fr >> 2) % 56, db = (fr >> 2) / 56;
      const int q = ks / 7, jj = ks % 7;
      const int d = 64 * db + 16 * (n >> 2) + 4 * dt + (n & 3);
      int flo, fhi;
      if (jj < 6) { flo = 112 * (2 * q + (jj >= 3)) + 32 * (jj % 3) + 4 * g; fhi = flo + 16; }
      else { flo = 224 * q + 96 + 4 * g; fhi = flo + 112; }
      u16 v[8];
#pragma unroll
      for (int e = 0; e < 4; ++e) { v[e] = w2[(size_t)(flo + e) * D + d]; v[4 + e] = w2[(size_t)(fhi + e) * D + d]; }
      u32x4 o4;
#pragma unroll
      for (int h = 0; h < 4; ++h) o4[h] = (unsigned)v[2 * h] | ((unsigned)v[2 * h + 1] << 16);
      *(u32x4*)(ws + OFF_W2 + ((size_t)fr * 64 + lane) * 16) = o4;
    }
  }
}

__global__ __launch_bounds__(256) void k2_colfft(const u16* __restrict__ w1, const u16* __restrict__ w2, char* __restrict__ ws) {
  __shared__ __attribute__((aligned(16))) char lds[8192];
  const int lane = threadIdx.x & 63, w = __builtin_amdgcn_readfirstlane(threadIdx.x >> 6);
  if (blockIdx.x >= DF) { retile_weights(w1, w2, ws, blockIdx.x - DF, w, lane); return; }
  const int col = blockIdx.x;
  const int j = lane & 15, g = lane >> 4;
  const char* zc = ws + OFF_Z + (size_t)col * COLB;
  u32x4 za[4][2];
#pragma unroll
  for (int t = 0; t < 4; ++t)
#pragma unroll
    for (int bt = 0; bt < 2; ++bt) za[t][bt] = *(const u32x4*)(zc + (((4 * t + g) * 32 + 16 * bt + j) * 16));
  const char* wc = ws + OFF_W + (size_t)col * COLB;
  u32x4 wv[2];
#pragma unroll
  for (int kt = 0; kt < 2; ++kt) wv[kt] = *(const u32x4*)(wc + (((4 * kt + g) * 64 + 16 * w + j) * 16));
  u32x4 F1R[4], F1I[4], F2R[2][2], F2I[2][2];
  float fc[2][4], fs[2][4], ic[2][4], is[2][4];
#pragma unroll
  for (int t = 0; t < 4; ++t)
#pragma unroll
    for (int h = 0; h < 4; ++h) {
      const float ang = (float)(((16 * t + 4 * g + h) * (16 * w + j)) & 63) * (1.f / 64.f);
      const float c = cosr(ang), s = sinr(ang);
      F1R[t][h] = pk(c, s);
      F1I[t][h] = pk(-s, c);
    }
#pragma unroll
  for (int kt = 0; kt < 2; ++kt)
#pragma unroll
    for (int bt = 0; bt < 2; ++bt)
#pragma unroll
      for (int h = 0; h < 4; ++h) {
        const float ang = (float)(((16 * bt + 4 * g + h) * (16 * kt + j)) & 31) * (1.f / 32.f);
        const float c = cosr(ang), s = sinr(ang);
        F2R[kt][bt][h] = pk(c, s);
        F2I[kt][bt][h] = pk(-s, c);
      }
#pragma unroll
  for (int bt = 0; bt < 2; ++bt)
#pragma unroll
    for (int h = 0; h < 4; ++h) {
      const float a1 = (float)((16 * bt + 4 * g + h) * (16 * w + j)) * (1.f / 2048.f);
      fc[bt][h] = cosr(a1);
      fs[bt][h] = sinr(a1);
      const float a2 = (float)((16 * bt + j) * (16 * w + 4 * g + h)) * (1.f / 2048.f);
      ic[bt][h] = cosr(a2) * (1.f / 1048576.f);
      is[bt][h] = sinr(a2) * (1.f / 1048576.f);
    }
  const f32x4 z = {0.f, 0.f, 0.f, 0.f};
  u32x4 tf[2];
#pragma unroll
  for (int bt = 0; bt < 2; ++bt) {
    f32x4 re = z, im = z;
#pragma unroll
    for (int t = 0; t < 4; ++t) { re = mfma(za[t][bt], F1R[t], re); im = mfma(za[t][bt], F1I[t], im); }
#pragma unroll
    for (int h = 0; h < 4; ++h) tf[bt][h] = pk(re[h] * fc[bt][h] + im[h] * fs[bt][h], im[h] * fc[bt][h] - re[h] * fs[bt][h]);
  }
  u32x4 yf[2];
#pragma unroll
  for (int kt = 0; kt < 2; ++kt) {
    f32x4 re = z, im = z;
#pragma unroll
    for (int bt = 0; bt < 2; ++bt) { re = mfma(F2R[kt][bt], tf[bt], re); im = mfma(F2I[kt][bt], tf[bt], im); }
#pragma unroll
    for (int h = 0; h < 4; ++h) {
      const float a = bflo(wv[kt][h]), b = bfhi(wv[kt][h]);
      yf[kt][h] = pk(re[h] * b + im[h] * a, re[h] * a - im[h] * b);
    }
  }
  u32x4 uf[2];
#pragma unroll
  for (int bt = 0; bt < 2; ++bt) {
    f32x4 re = z, im = z;
#pragma unroll
    for (int kt = 0; kt < 2; ++kt) { im = mfma(yf[kt], F2R[bt][kt], im); re = mfma(yf[kt], F2I[bt][kt], re); }
#pragma unroll
    for (int h = 0; h < 4; ++h) uf[bt][h] = pk(im[h] * ic[bt][h] + re[h] * is[bt][h], re[h] * ic[bt][h] - im[h] * is[bt][h]);
    *(u32x4*)(lds + ((w * 2 + bt) * 64 + lane) * 16) = uf[bt];
  }
  __syncthreads();
  char* yc = ws + OFF_Y + (size_t)col * COLB;
#pragma unroll
  for (int bt = 0; bt < 2; ++bt) {
    f32x4 re = z, im = z;
#pragma unroll
    for (int t = 0; t < 4; ++t) {
      const u32x4 u = *(const u32x4*)(lds + ((t * 2 + bt) * 64 + lane) * 16);
      im = mfma(F1R[t], u, im);
      re = mfma(F1I[t], u, re);
    }
    u32x4 o;
#pragma unroll
    for (int h = 0; h < 4; ++h) o[h] = pk(re[h], im[h]);
    *(u32x4*)(yc + (((4 * w + g) * 32 + 16 * bt + j) * 16)) = o;
  }
}

__global__ __launch_bounds__(256) void k3_irfft_ln(const u16* __restrict__ x, char* __restrict__ ws, u16* __restrict__ out) {
  const int lane = threadIdx.x & 63, wave = __builtin_amdgcn_readfirstlane(threadIdx.x >> 6);
  const int xcd = blockIdx.x & 7, bi = blockIdx.x >> 3;
  const int j = lane & 15, g = lane >> 4;
  const int P = 2 * xcd + (bi >> 4), b0 = 2 * (bi & 15) + (wave >> 1), r0 = 2 * (wave & 1);
  const char* yb = ws + OFF_Y + (size_t)(P * 32 + b0) * 16 + r0 * 4;
  u32x2 ld[4];
#pragma unroll
  for (int h = 0; h < 4; ++h) ld[h] = *(const u32x2*)(yb + (size_t)kcol(j, 4 * g + h) * COLB);
  const u32x2 nyq = *(const u32x2*)(yb + (size_t)256 * COLB);
  u32x2 xv[2][2];
#pragma unroll
  for (int r = 0; r < 2; ++r)
#pragma unroll
    for (int at = 0; at < 2; ++at)
      xv[r][at] = *(const u32x2*)(x + (size_t)(128 * P + 32 * (r0 + r) + b0) * D + 256 * at + 16 * j + 4 * g);
  u32x4 BR, BI, SR, SI, C2[2];
  float tc[4], ts[4];
#pragma unroll
  for (int h = 0; h < 4; ++h) {
    const int k2 = 4 * g + h;
    const float th = (float)((j * k2) & 15) * (1.f / 16.f);
    const float ct = cosr(th), st = sinr(th);
    const float ph = (float)((j * (2 * k2 - 15)) & 31) * (1.f / 32.f);
    const float cp = cosr(ph), sp = sinr(ph);
    BR[h] = k2 < 8 ? pk(ct, -st) : pk(ct, st);
    BI[h] = k2 < 8 ? pk(st, ct) : pk(st, -ct);
    SR[h] = k2 == 0 ? pk(1.f, (j & 1) ? -1.f : 1.f) : (k2 < 8 ? pk(2.f * ct, -2.f * st) : 0u);
    SI[h] = k2 < 8 ? 0u : pk(2.f * cp, -2.f * sp);
    const float tw = (float)(j * k2) * (1.f / 512.f);
    tc[h] = cosr(tw);
    ts[h] = sinr(tw);
#pragma unroll
    for (int at = 0; at < 2; ++at) {
      const float an = (float)(((16 * at + j) * k2) & 31) * (1.f / 32.f);
      C2[at][h] = k2 == 0 ? pk(1.f, (j & 1) ? -1.f : 1.f) : pk(2.f * cosr(an), -2.f * sinr(an));
    }
  }
  const f32x4 z = {0.f, 0.f, 0.f, 0.f};
#pragma unroll
  for (int r = 0; r < 2; ++r) {
    u32x4 a = {ld[0][r], ld[1][r], ld[2][r], ld[3][r]};
    if (lane == 0) a[0] = (a[0] & 0xffffu) | (nyq[r] << 16);
    f32x4 ur = mfma(a, BR, z), ui = mfma(a, BI, z);
    const f32x4 sr = mfma(a, SR, z), si = mfma(a, SI, z);
    if (g == 0) { ur[0] = sr[0]; ui[0] = si[0]; }
    u32x4 t;
#pragma unroll
    for (int h = 0; h < 4; ++h) t[h] = pk(ur[h] * tc[h] - ui[h] * ts[h], ui[h] * tc[h] + ur[h] * ts[h]);
    float v[8], o[8];
    u32x2 hv[2];
#pragma unroll
    for (int at = 0; at < 2; ++at) {
      const f32x4 y = mfma(t, C2[at], z);
      hv[at][0] = pk(bflo(xv[r][at][0]) + rbf(y[0]), bfhi(xv[r][at][0]) + rbf(y[1]));
      hv[at][1] = pk(bflo(xv[r][at][1]) + rbf(y[2]), bfhi(xv[r][at][1]) + rbf(y[3]));
      v[4 * at] = bflo(hv[at][0]); v[4 * at + 1] = bfhi(hv[at][0]);
      v[4 * at + 2] = bflo(hv[at][1]); v[4 * at + 3] = bfhi(hv[at][1]);
      *(u32x2*)(out + (size_t)(128 * P + 32 * (r0 + r) + b0) * D + 256 * at + 16 * j + 4 * g) = hv[at];
    }
    layernorm8(v, o);
    u32x4 u;
#pragma unroll
    for (int h = 0; h < 4; ++h) u[h] = pk(o[2 * h], o[2 * h + 1]);
    const int rho = 128 * P + 4 * b0 + r0 + r;
    *(u32x4*)(ws + OFF_A1 + ((size_t)(((rho >> 4) * 16 + (lane >> 2)) * 64 + (lane & 3) * 16 + (rho & 15))) * 16) = u;
  }
}

DI float silu_bf(float v) {
  const float h = rbf(v);
  return h * __builtin_amdgcn_rcpf(1.f + __builtin_amdgcn_exp2f(-1.44269504088896f * h));
}

__global__ __launch_bounds__(256) void g1_gemm(char* __restrict__ ws) {
  __shared__ __attribute__((aligned(16))) char lds[114688];
  const int lane = threadIdx.x & 63, w = __builtin_amdgcn_readfirstlane(threadIdx.x >> 6);
  const int xcd = blockIdx.x & 7, bi = blockIdx.x >> 3;
  const int P = 2 * xcd + (bi >> 4), cb = bi & 15;
  const char* wsrc = ws + OFF_W1 + (size_t)cb * 114688 + lane * 16;
#pragma unroll
  for (int i = 0; i < 28; ++i)
    __builtin_amdgcn_global_load_lds((const __attribute__((address_space(1))) void*)(wsrc + (i * 4 + w) * 1024),
                                     (LDSP void*)(lds + (i * 4 + w) * 1024), 16, 0, 0);
  const char* asrc = ws + OFF_A1 + (size_t)(8 * P + 2 * w) * 16384 + lane * 16;
  u32x4 a[2][16];
#pragma unroll
  for (int ks = 0; ks < 16; ++ks)
#pragma unroll
    for (int m = 0; m < 2; ++m) a[m][ks] = *(const u32x4*)(asrc + m * 16384 + ks * 1024);
  f32x4 acc[2][7];
#pragma unroll
  for (int m = 0; m < 2; ++m)
#pragma unroll
    for (int t = 0; t < 7; ++t) acc[m][t] = (f32x4){0.f, 0.f, 0.f, 0.f};
  asm volatile("s_waitcnt vmcnt(0)" ::: "memory");
  __syncthreads();
#pragma unroll
  for (int ks = 0; ks < 16; ++ks)
#pragma unroll
    for (int t = 0; t < 7; ++t) {
      const u32x4 b = *(const u32x4*)(lds + (ks * 7 + t) * 1024 + lane * 16);
#pragma unroll
      for (int m = 0; m < 2; ++m) acc[m][t] = mfma(b, a[m][ks], acc[m][t]);
    }
  const int ksb = 7 * (cb >> 1) + 3 * (cb & 1);
#pragma unroll
  for (int m = 0; m < 2; ++m) {
    char* hb = ws + OFF_H + (size_t)(8 * P + 2 * w + m) * 57344 + lane * 16;
    u32x2 p[7];
#pragma unroll
    for (int t = 0; t < 7; ++t) {
      p[t][0] = pk(silu_bf(acc[m][t][0]), silu_bf(acc[m][t][1]));
      p[t][1] = pk(silu_bf(acc[m][t][2]), silu_bf(acc[m][t][3]));
    }
#pragma unroll
    for (int u = 0; u < 3; ++u) *(u32x4*)(hb + (ksb + u) * 1024) = (u32x4){p[2 * u][0], p[2 * u][1], p[2 * u + 1][0], p[2 * u + 1][1]};
    *(u32x2*)(hb + (7 * (cb >> 1) + 6) * 1024 + (cb & 1) * 8) = p[6];
  }
}

__global__ __launch_bounds__(256) void g2_gemm(const char* __restrict__ ws, u16* __restrict__ out) {
  __shared__ __attribute__((aligned(16))) char lds[65536];
  const int lane = threadIdx.x & 63, w = __builtin_amdgcn_readfirstlane(threadIdx.x >> 6);
  const int xcd = blockIdx.x & 7, bi = blockIdx.x >> 3;
  const int rb = 4 * xcd + (bi >> 3), db = bi & 7;
  const int i = lane & 15, g = lane >> 4;
  const int rt = 4 * rb + w;
  const int srow = 128 * (rt >> 3) + 32 * (i & 3) + 4 * (rt & 7) + (i >> 2);
  u16* op = out + (size_t)srow * D + 64 * db + 16 * g;
  const u32x4 h0 = *(const u32x4*)op, h1 = *(const u32x4*)(op + 8);
  const char* bsrc = ws + OFF_W2 + ((size_t)db * 56 + 14 * w) * 4096 + lane * 16;
  const char* asrc = ws + OFF_H + (size_t)(4 * rb) * 57344 + (size_t)(14 * w) * 1024 + lane * 16;
  f32x4 acc[4][4];
#pragma unroll
  for (int m = 0; m < 4; ++m)
#pragma unroll
    for (int t = 0; t < 4; ++t) acc[m][t] = (f32x4){0.f, 0.f, 0.f, 0.f};
#pragma unroll
  for (int ks = 0; ks < 14; ++ks) {
    u32x4 a[4], b[4];
#pragma unroll
    for (int m = 0; m < 4; ++m) { a[m] = *(const u32x4*)(asrc + m * 57344 + ks * 1024); b[m] = *(const u32x4*)(bsrc + ks * 4096 + m * 1024); }
#pragma unroll
    for (int m = 0; m < 4; ++m)
#pragma unroll
      for (int t = 0; t < 4; ++t) acc[m][t] = mfma(b[t], a[m], acc[m][t]);
  }
#pragma unroll
  for (int m = 0; m < 4; ++m)
#pragma unroll
    for (int t = 0; t < 4; ++t) *(f32x4*)(lds + (((w * 4 + m) * 4 + t) * 64 + lane) * 16) = acc[m][t];
  __syncthreads();
  unsigned o[8];
#pragma unroll
  for (int t = 0; t < 4; ++t) {
    f32x4 s = *(const f32x4*)(lds + (((0 * 4 + w) * 4 + t) * 64 + lane) * 16);
#pragma unroll
    for (int v = 1; v < 4; ++v) s += *(const f32x4*)(lds + (((v * 4 + w) * 4 + t) * 64 + lane) * 16);
    const unsigned ha = t < 2 ? h0[2 * t] : h1[2 * t - 4], hb = t < 2 ? h0[2 * t + 1] : h1[2 * t - 3];
    o[2 * t] = pk(bflo(ha) + rbf(s[0]), bfhi(ha) + rbf(s[1]));
    o[2 * t + 1] = pk(bflo(hb) + rbf(s[2]), bfhi(hb) + rbf(s[3]));
  }
  *(u32x4*)op = (u32x4){o[0], o[1], o[2], o[3]};
  *(u32x4*)(op + 8) = (u32x4){o[4], o[5], o[6], o[7]};
}

extern "C" void kernel_launch(void* const* d_in, const int* in_sizes, int n_in, void* d_out, int out_size, void* d_ws,
                              size_t ws_size, hipStream_t stream) {
  const u16* x = (const u16*)d_in[0];
  const u16* wr = (const u16*)d_in[1];
  const u16* wi = (const u16*)d_in[2];
  const u16* w1 = (const u16*)d_in[4];
  const u16* w2 = (const u16*)d_in[5];
  char* ws = (char*)d_ws;
  u16* out = (u16*)d_out;
  k1_ln_rfft<<<256, 256, 0, stream>>>(x, wr, wi, ws);
  k2_colfft<<<DF + 256, 256, 0, stream>>>(w1, w2, ws);
  k3_irfft_ln<<<256, 256, 0, stream>>>(x, ws, out);
  g1_gemm<<<256, 256, 0, stream>>>(ws);
  g2_gemm<<<256, 256, 0, stream>>>(ws, out);
}
```

```cpp
#include <hip/hip_runtime.h>
#include <hip/hip_bf16.h>

typedef __bf16 bf16x8 __attribute__((ext_vector_type(8)));
typedef __bf16 bf16x2 __attribute__((ext_vector_type(2)));
typedef float f32x2 __attribute__((ext_vector_type(2)));
typedef float f32x4 __attribute__((ext_vector_type(4)));
typedef short s16x4 __attribute__((ext_vector_type(4)));
typedef unsigned u32x2 __attribute__((ext_vector_type(2)));
typedef unsigned u32x2u __attribute__((ext_vector_type(2), aligned(2)));
typedef unsigned u32x4 __attribute__((ext_vector_type(4)));
typedef unsigned short u16;
typedef __amdgpu_buffer_rsrc_t rsrc_t;
#define DI __device__ __forceinline__
#define LDSP __attribute__((address_space(3)))
#define SCHED() __builtin_amdgcn_sched_barrier(0)
#define BAR() asm volatile("s_waitcnt lgkmcnt(0)\n\ts_barrier" ::: "memory")

constexpr int S = 2048, D = 512, DF = 257, F = 1792;
constexpr unsigned COLB = 8192;
constexpr unsigned OFF_Z = 0;
constexpr unsigned OFF_W = OFF_Z + 256 * COLB;
constexpr unsigned OFF_Y = OFF_W + 260 * COLB;
constexpr unsigned OFF_A1 = OFF_Y + 256 * COLB;
constexpr unsigned OFF_HR = OFF_A1 + 2097152;
constexpr unsigned OFF_W1 = OFF_HR + 2097152;
constexpr unsigned OFF_W2 = OFF_W1 + 1835008;
constexpr unsigned OFF_H = OFF_W2 + 1835008;
constexpr unsigned WS_USED = OFF_H + 7340032;
constexpr unsigned MARK = 0xffffffffu;
constexpr unsigned SS = 0x80008000u;
constexpr int SPIN_MAX = 1 << 17;

#define ST_DECL
#define ST(n)
#define ST_END(kid)

DI unsigned pk(float lo, float hi) {
  f32x2 f = {lo, hi};
  return __builtin_bit_cast(unsigned, __builtin_convertvector(f, bf16x2));
}
DI float bflo(unsigned v) { return __builtin_bit_cast(float, v << 16); }
DI float bfhi(unsigned v) { return __builtin_bit_cast(float, v & 0xffff0000u); }
DI float rbf(float x) { return bflo(pk(x, x)); }
DI f32x4 mfma(u32x4 a, u32x4 b, f32x4 c) {
  return __builtin_amdgcn_mfma_f32_16x16x32_bf16(__builtin_bit_cast(bf16x8, a), __builtin_bit_cast(bf16x8, b), c, 0, 0, 0);
}
DI u32x4 pk4(f32x4 lo, f32x4 hi) { return (u32x4){pk(lo[0], hi[0]), pk(lo[1], hi[1]), pk(lo[2], hi[2]), pk(lo[3], hi[3])}; }
DI float cosr(float r) { return __builtin_amdgcn_cosf(r); }
DI float sinr(float r) { return __builtin_amdgcn_sinf(r); }
DI rsrc_t make_rsrc(const void* p, unsigned bytes) { return __builtin_amdgcn_make_buffer_rsrc((void*)p, 0, bytes, 0x00020000); }
template <int AUX> DI u32x4 ld16(rsrc_t r, unsigned voff, unsigned soff) { return __builtin_amdgcn_raw_buffer_load_b128(r, voff, soff, AUX); }
template <int AUX> DI u32x2 ld8(rsrc_t r, unsigned voff, unsigned soff) { return __builtin_amdgcn_raw_buffer_load_b64(r, voff, soff, AUX); }
template <int AUX> DI void st16(u32x4 v, rsrc_t r, unsigned voff, unsigned soff) { __builtin_amdgcn_raw_buffer_store_b128(v, r, voff, soff, AUX); }
template <int AUX> DI void st8(u32x2 v, rsrc_t r, unsigned voff, unsigned soff) { __builtin_amdgcn_raw_buffer_store_b64(v, r, voff, soff, AUX); }
DI u32x2 trread(const char* p) { return __builtin_bit_cast(u32x2, __builtin_amdgcn_ds_read_tr16_b64_v4i16((LDSP s16x4*)p)); }
DI unsigned umax3(unsigned a, unsigned b, unsigned c) { return max(max(a, b), c); }
DI unsigned mark4(unsigned m, u32x4 v) { return umax3(umax3(m, v[0], v[1]), v[2], v[3]); }

template <int CTRL>
DI float dpp_add(float v) {
  return v + __builtin_bit_cast(float, __builtin_amdgcn_update_dpp(0, __builtin_bit_cast(int, v), CTRL, 0xf, 0xf, true));
}
DI float wave_sum(float v) {
  v = dpp_add<0xB1>(v);
  v = dpp_add<0x4E>(v);
  v = dpp_add<0x141>(v);
  v = dpp_add<0x140>(v);
  const int iv = __builtin_bit_cast(int, v);
  const float a = __builtin_bit_cast(float, __builtin_amdgcn_readlane(iv, 0));
  const float b = __builtin_bit_cast(float, __builtin_amdgcn_readlane(iv, 16));
  const float c = __builtin_bit_cast(float, __builtin_amdgcn_readlane(iv, 32));
  const float d = __builtin_bit_cast(float, __builtin_amdgcn_readlane(iv, 48));
  return (a + b) + (c + d);
}
DI u32x4 layernorm8(u32x4 p) {
  const f32x4 lo = {bflo(p[0]), bflo(p[1]), bflo(p[2]), bflo(p[3])}, hi = {bfhi(p[0]), bfhi(p[1]), bfhi(p[2]), bfhi(p[3])};
  const f32x4 s4 = lo + hi, q4 = lo * lo + hi * hi;
  const float s = wave_sum((s4[0] + s4[1]) + (s4[2] + s4[3])), q = wave_sum((q4[0] + q4[1]) + (q4[2] + q4[3]));
  const float mean = s * (1.f / 512.f);
  const float var = fmaxf(q * (1.f / 512.f) - mean * mean, 0.f);
  const float rs = __builtin_amdgcn_rcpf(rbf(__builtin_amdgcn_sqrtf(rbf(rbf(var) + 1e-5f))));
  const float nb = -rbf(mean) * rs;
  return pk4(lo * rs + nb, hi * rs + nb);
}
DI int kcol(int k1, int k2) {
  if (k1 == 0) return k2 < 8 ? 32 * k2 : 32 * k2 - 240;
  return k2 < 8 ? k1 + 32 * k2 : 512 - k1 - 32 * k2;
}

constexpr int K1_FFT = 256, K1_WT = 64;
__global__ __launch_bounds__(512, 2) void k1_ln_rfft(const u16* __restrict__ x, const u16* __restrict__ wr,
                                                     const u16* __restrict__ wi, char* __restrict__ ws) {
  __shared__ __attribute__((aligned(16))) char lds[24576];
  const int lane = threadIdx.x & 63, v = __builtin_amdgcn_readfirstlane(threadIdx.x >> 6);
  const int xcd = blockIdx.x & 7;
  const int j = lane & 15, g = lane >> 4;
  const rsrc_t rw = make_rsrc(ws, WS_USED);
  ST_DECL; ST(0);
  if (blockIdx.x < K1_FFT) {
    const int bi = blockIdx.x >> 3, P = 2 * xcd + (bi >> 4), c = bi & 15;
    const u32x4 xv = *(const u32x4*)(x + (unsigned)((128 * P + 32 * (v & 3) + 2 * c + (v >> 2)) * D) + 8 * lane);
    st16<0>((u32x4){MARK, MARK, MARK, MARK}, rw, threadIdx.x * 16, OFF_A1 + blockIdx.x * 8192);
    char* ct = lds + 8192;
    {
      u32x4 o;
#pragma unroll
      for (int h = 0; h < 4; ++h) {
        const int b = 4 * g + h;
        if (v < 2) {
          const int a0 = 4 * g + ((2 * h) & 3) + 16 * (h >> 1);
          const int jj = (v == 1 && j == 0) ? 16 : j;
          const float sh = (v == 1 && j != 0) ? 0.25f : 0.f;
          o[h] = pk(cosr((float)((a0 * jj) & 31) * (1.f / 32.f) + sh), cosr((float)(((a0 + 1) * jj) & 31) * (1.f / 32.f) + sh));
        } else if (v < 4) {
          const float th = (float)((b * j) & 15) * (1.f / 16.f);
          const float cs = cosr(th), sn = sinr(th);
          o[h] = v == 2 ? pk(cs, sn) : (pk(-sn, cs) ^ (j < 8 ? 0u : SS));
        } else if (v < 6) {
          const int m = j < 8 ? ((v == 5 && j == 0) ? 16 : 2 * j) : 2 * j - 15;
          const float val = cosr((float)((b * m) & 31) * (1.f / 32.f) + ((v == 5 && j != 0) ? 0.25f : 0.f));
          o[h] = j < 8 ? pk(val, 0.f) : pk(0.f, val);
        } else {
          o[h] = __builtin_bit_cast(unsigned, cosr((float)(b * j) * (1.f / 512.f) - (v == 7 ? 0.25f : 0.f)));
        }
      }
      *(u32x4*)(ct + v * 1024 + lane * 16) = o;
    }
    BAR();
    const u32x4 BR = *(const u32x4*)(ct + lane * 16), BI = *(const u32x4*)(ct + 1024 + lane * 16);
    const u32x4 AR = *(const u32x4*)(ct + 2048 + lane * 16), AI = *(const u32x4*)(ct + 3072 + lane * 16);
    const u32x4 SR = *(const u32x4*)(ct + 4096 + lane * 16), SI = *(const u32x4*)(ct + 5120 + lane * 16);
    const f32x4 tc = *(const f32x4*)(ct + 6144 + lane * 16), ts = *(const f32x4*)(ct + 7168 + lane * 16);
    char* so = lds + 16384 + v * 1024;
    int oc[4];
#pragma unroll
    for (int h = 0; h < 4; ++h) oc[h] = kcol(j, 4 * g + h) * 4;
    ST(1);
    char* wl = lds + v * 1024;
    *(u32x4*)(wl + lane * 16) = layernorm8(xv);
    ST(2);
    const u32x2 a0 = trread(wl + lane * 8), a1 = trread(wl + 512 + lane * 8);
    const u32x4 a = {a0[0], a0[1], a1[0], a1[1]};
    const f32x4 z = {0.f, 0.f, 0.f, 0.f};
    const f32x4 dr = mfma(a, BR, z), di = mfma(a, BI, z);
    const u32x4 t = pk4(dr * tc + di * ts, di * tc - dr * ts);
    const f32x4 xr = mfma(AR, t, z), xi = mfma(AI, t, z), sr = mfma(SR, t, z), si = mfma(SI, t, z);
    const u32x4 o1 = pk4(xr, xi), o2 = pk4(sr, si);
#pragma unroll
    for (int h = 0; h < 4; ++h) *(unsigned*)(so + oc[h]) = j == 0 ? o2[h] : o1[h];
    ST(3);
    BAR();
    const char* si_ = lds + 16384 + (threadIdx.x & 1) * 4096 + (threadIdx.x >> 1) * 4;
    const u32x4 o = {*(const unsigned*)si_, *(const unsigned*)(si_ + 1024), *(const unsigned*)(si_ + 2048), *(const unsigned*)(si_ + 3072)};
    st16<0>(o, rw, (threadIdx.x >> 1) * COLB + (threadIdx.x & 1) * 16, OFF_Z + (P * 32 + 2 * c) * 16);
    ST(4);
  } else {
    const int c = 64 * xcd + 8 * ((blockIdx.x - K1_FFT) >> 3) + v;
    const int p0 = (c & 63) + 256 * (c >> 6);
    u32x2 a[4], b[4];
#pragma unroll
    for (int r = 0; r < 4; ++r) {
      const unsigned ro = (unsigned)(p0 + 64 * r) * DF;
      a[r] = *(const u32x2u*)(wr + ro + 4 * lane);
      b[r] = *(const u32x2u*)(wi + ro + 4 * lane);
    }
    const int pe = p0 + 64 * (lane & 3), pm = (S - pe) & (S - 1);
    const float ar = bflo(wr[pe * DF]), ai = bflo(wi[pe * DF]), br = bflo(wr[pe * DF + 256]), bi = bflo(wi[pe * DF + 256]);
    const float cr = bflo(wr[pm * DF]), ci = bflo(wi[pm * DF]), dr = bflo(wr[pm * DF + 256]), di = bflo(wi[pm * DF + 256]);
#pragma unroll
    for (int q = 0; q < 4; ++q) {
      u32x4 o;
#pragma unroll
      for (int r = 0; r < 4; ++r)
        o[r] = __builtin_amdgcn_perm(b[r][q >> 1], a[r][q >> 1], (q & 1) ? 0x07060302u : 0x05040100u);
      if (q > 0 || lane > 0) st16<0>(o, rw, (4 * lane + q) * COLB, OFF_W + c * 16);
    }
    if (lane < 4) {
      const float Ar = ar + cr, Ai = ai - ci, Br = br + dr, Bi = bi - di;
      *(unsigned*)(ws + OFF_W + c * 16 + lane * 4) = pk(0.25f * (Ar + Br), 0.25f * (Ai + Bi));
      *(unsigned*)(ws + OFF_W + 256 * COLB + c * 16 + lane * 4) = pk(0.25f * (Ar - Br), 0.25f * (Ai - Bi));
    }
  }
  ST_END(0);
}

constexpr int K2_FFT = 256, K2_W1 = 64, K2_W2 = 112;
__global__ __launch_bounds__(256, 2) void k2_colfft(const u16* __restrict__ w1, const u16* __restrict__ w2, char* __restrict__ ws) {
  __shared__ __attribute__((aligned(16))) char lds[28672];
  const int lane = threadIdx.x & 63, w = __builtin_amdgcn_readfirstlane(threadIdx.x >> 6);
  const int j = lane & 15, g = lane >> 4;
  const rsrc_t rw = make_rsrc(ws, WS_USED);
  ST_DECL; ST(0);
  if (blockIdx.x >= K2_FFT + K2_W1) {
    const int u = (blockIdx.x - K2_FFT - K2_W1) * 4 + w, db = u & 7, ks = u >> 3;
    const int q = ks / 7, jj = ks - 7 * q;
    const int flo = jj < 6 ? 224 * q + 112 * (jj >= 3) + 32 * (jj % 3) : 224 * q + 96, fd = jj < 6 ? 16 : 112;
    char* wl = lds + w * 4096;
    u32x4 t[4];
#pragma unroll
    for (int i = 0; i < 4; ++i) {
      const int r = 8 * (i & 1) + (lane >> 3);
      t[i] = *(const u32x4*)(w2 + (unsigned)((flo + (i >> 1) * fd + r) * D + 64 * db + 8 * (lane & 7)));
    }
#pragma unroll
    for (int i = 0; i < 4; ++i) *(u32x4*)(wl + i * 1024 + lane * 16) = t[i];
    const char* rp = wl + (4 * g + (j >> 2)) * 128 + (j & 3) * 32;
#pragma unroll
    for (int dt = 0; dt < 4; ++dt) {
      const u32x2 lo = trread(rp + dt * 8), hi = trread(rp + 2048 + dt * 8);
      st16<0>((u32x4){lo[0], lo[1], hi[0], hi[1]}, rw, lane * 16 + dt * 1024, OFF_W2 + (db * 56 + ks) * 4096);
    }
    ST_END(1);
    return;
  }
  if (blockIdx.x >= K2_FFT) {
    const int u = (blockIdx.x - K2_FFT) * 4 + w, cb = u & 15, ks = u >> 4;
    char* wl = lds + w * 7168;
    u32x4 t[7];
#pragma unroll
    for (int i = 0; i < 7; ++i) {
      const int e = i * 64 + lane, r = e / 14, ch = e - 14 * r;
      const int row = 64 * (ks & 3) + 4 * (ks >> 2) + 16 * ((r >> 2) & 3) + (r & 3) + 256 * (r >> 4);
      t[i] = *(const u32x4*)(w1 + (unsigned)(row * F + 112 * cb + 8 * ch));
    }
#pragma unroll
    for (int i = 0; i < 7; ++i) *(u32x4*)(wl + i * 1024 + lane * 16) = t[i];
    const char* rp = wl + (4 * g + (j >> 2)) * 224 + (j & 3) * 8;
#pragma unroll
    for (int tt = 0; tt < 7; ++tt) {
      const u32x2 lo = trread(rp + tt * 32), hi = trread(rp + 3584 + tt * 32);
      st16<0>((u32x4){lo[0], lo[1], hi[0], hi[1]}, rw, lane * 16 + tt * 1024, OFF_W1 + (cb * 16 + ks) * 7168);
    }
    ST_END(1);
    return;
  }
  const int col = blockIdx.x;
  u32x4 za[4][2];
#pragma unroll
  for (int t = 0; t < 4; ++t)
#pragma unroll
    for (int bt = 0; bt < 2; ++bt) za[t][bt] = ld16<0>(rw, (g * 32 + j) * 16 + t * 2048 + bt * 256, OFF_Z + col * COLB);
  u32x4 wv[2], wv2[2];
#pragma unroll
  for (int kt = 0; kt < 2; ++kt) wv[kt] = ld16<0>(rw, (g * 64 + j) * 16, OFF_W + col * COLB + kt * 4096 + w * 256);
  if (col == 0) {
#pragma unroll
    for (int kt = 0; kt < 2; ++kt) wv2[kt] = ld16<0>(rw, (g * 64 + j) * 16, OFF_W + 256 * COLB + kt * 4096 + w * 256);
  }
  u32x4 F1R[4], F1I[4], F2R[2][2], F2I[2][2];
  f32x4 fc[2], fs[2], ic[2], is[2];
  {
    const int k1 = 16 * w + j;
    const unsigned m1 = (j & 1) ? SS : 0u, mr = (j & 2) ? SS : 0u, mi = ((j + 1) & 2) ? SS : 0u;
#pragma unroll
    for (int h = 0; h < 4; ++h) {
      const float ang = (float)(((4 * g + h) * k1) & 63) * (1.f / 64.f);
      const float c = cosr(ang), s = sinr(ang);
      const unsigned r0 = pk(c, s), i0 = pk(-s, c);
      F1R[0][h] = r0; F1I[0][h] = i0;
      F1R[2][h] = r0 ^ m1; F1I[2][h] = i0 ^ m1;
      F1R[1][h] = ((j & 1) ? i0 : r0) ^ mr; F1I[1][h] = ((j & 1) ? r0 : i0) ^ mi;
      F1R[3][h] = F1R[1][h] ^ m1; F1I[3][h] = F1I[1][h] ^ m1;
      const float an2 = (float)(((4 * g + h) * j) & 31) * (1.f / 32.f);
      const float c2 = cosr(an2), s2 = sinr(an2);
      const unsigned r2 = pk(c2, s2), i2 = pk(-s2, c2), mh = (h & 1) ? SS : 0u;
      F2R[0][0][h] = r2; F2I[0][0][h] = i2;
      F2R[0][1][h] = r2 ^ m1; F2I[0][1][h] = i2 ^ m1;
      F2R[1][0][h] = r2 ^ mh; F2I[1][0][h] = i2 ^ mh;
      F2R[1][1][h] = r2 ^ m1 ^ mh; F2I[1][1][h] = i2 ^ m1 ^ mh;
#pragma unroll
      for (int bt = 0; bt < 2; ++bt) {
        const float a1 = (float)((16 * bt + 4 * g + h) * k1) * (1.f / 2048.f);
        fc[bt][h] = cosr(a1);
        fs[bt][h] = sinr(a1);
        const float a2 = (float)((16 * bt + j) * (16 * w + 4 * g + h)) * (1.f / 2048.f);
        ic[bt][h] = cosr(a2);
        is[bt][h] = sinr(a2);
      }
    }
  }
  ST(1);
  const f32x4 z = {0.f, 0.f, 0.f, 0.f};
  f32x4 re[2] = {z, z}, im[2] = {z, z};
#pragma unroll
  for (int t = 0; t < 4; ++t)
#pragma unroll
    for (int bt = 0; bt < 2; ++bt) { re[bt] = mfma(za[t][bt], F1R[t], re[bt]); im[bt] = mfma(za[t][bt], F1I[t], im[bt]); }
  u32x4 tf[2];
#pragma unroll
  for (int bt = 0; bt < 2; ++bt) tf[bt] = pk4(re[bt] * fc[bt] + im[bt] * fs[bt], im[bt] * fc[bt] - re[bt] * fs[bt]);
  ST(2);
#pragma unroll
  for (int kt = 0; kt < 2; ++kt) {
    re[kt] = mfma(F2R[kt][1], tf[1], mfma(F2R[kt][0], tf[0], z));
    im[kt] = mfma(F2I[kt][1], tf[1], mfma(F2I[kt][0], tf[0], z));
  }
  u32x4 yf[2];
  f32x4 yr[2], yi[2];
#pragma unroll
  for (int kt = 0; kt < 2; ++kt) {
    const f32x4 a = {bflo(wv[kt][0]), bflo(wv[kt][1]), bflo(wv[kt][2]), bflo(wv[kt][3])};
    const f32x4 b = {bfhi(wv[kt][0]), bfhi(wv[kt][1]), bfhi(wv[kt][2]), bfhi(wv[kt][3])};
    yr[kt] = re[kt] * a - im[kt] * b;
    yi[kt] = re[kt] * b + im[kt] * a;
  }
  if (col == 0) {
    char* ex = lds + 8192;
    const int p0 = 16 * w + j + 256 * g;
#pragma unroll
    for (int kt = 0; kt < 2; ++kt)
#pragma unroll
      for (int h = 0; h < 4; ++h) *(f32x2*)(ex + p0 * 8 + (16 * kt + h) * 512) = (f32x2){re[kt][h], im[kt][h]};
    if (threadIdx.x == 0) *(f32x2*)(ex + 2048 * 8) = (f32x2){re[0][0], im[0][0]};
    BAR();
#pragma unroll
    for (int kt = 0; kt < 2; ++kt)
#pragma unroll
      for (int h = 0; h < 4; ++h) {
        const f32x2 m = *(const f32x2*)(ex + (2048 - 19 * 64 - p0) * 8 + (19 - 16 * kt - h) * 512);
        const float a = bflo(wv2[kt][h]), b = bfhi(wv2[kt][h]);
        yr[kt][h] += m[0] * a + m[1] * b;
        yi[kt][h] += m[0] * b - m[1] * a;
      }
  }
#pragma unroll
  for (int kt = 0; kt < 2; ++kt) yf[kt] = pk4(yi[kt], yr[kt]);
#pragma unroll
  for (int bt = 0; bt < 2; ++bt) {
    im[bt] = mfma(yf[1], F2R[bt][1], mfma(yf[0], F2R[bt][0], z));
    re[bt] = mfma(yf[1], F2I[bt][1], mfma(yf[0], F2I[bt][0], z));
  }
#pragma unroll
  for (int bt = 0; bt < 2; ++bt)
    *(u32x4*)(lds + ((w * 2 + bt) * 64 + lane) * 16) = pk4(im[bt] * ic[bt] + re[bt] * is[bt], re[bt] * ic[bt] - im[bt] * is[bt]);
  ST(3);
  BAR();
  ST(4);
  u32x4 u[4][2];
#pragma unroll
  for (int t = 0; t < 4; ++t)
#pragma unroll
    for (int bt = 0; bt < 2; ++bt) u[t][bt] = *(const u32x4*)(lds + ((t * 2 + bt) * 64 + lane) * 16);
  re[0] = re[1] = im[0] = im[1] = z;
#pragma unroll
  for (int t = 0; t < 4; ++t)
#pragma unroll
    for (int bt = 0; bt < 2; ++bt) { im[bt] = mfma(F1R[t], u[t][bt], im[bt]); re[bt] = mfma(F1I[t], u[t][bt], re[bt]); }
#pragma unroll
  for (int bt = 0; bt < 2; ++bt) st16<0>(pk4(re[bt], im[bt]), rw, (g * 32 + j) * 16 + bt * 256, OFF_Y + col * COLB + w * 2048);
  ST(5);
  ST_END(1);
}

DI f32x4 silu4(f32x4 h) {
  const f32x4 t = h * -1.44269504088896f;
  const f32x4 e = {__builtin_amdgcn_exp2f(t[0]), __builtin_amdgcn_exp2f(t[1]), __builtin_amdgcn_exp2f(t[2]), __builtin_amdgcn_exp2f(t[3])};
  const f32x4 d = e + 1.f;
  const f32x4 r = {__builtin_amdgcn_rcpf(d[0]), __builtin_amdgcn_rcpf(d[1]), __builtin_amdgcn_rcpf(d[2]), __builtin_amdgcn_rcpf(d[3])};
  return h * r;
}

constexpr int L_W1 = 0, L_Y = 114688, L_U = L_Y + 8704, L_END = L_U + 8192;
__global__ __launch_bounds__(256, 1) void k345(const u16* __restrict__ x, char* __restrict__ ws, u16* __restrict__ out) {
  __shared__ __attribute__((aligned(16))) char lds[L_END];
  const int tid = threadIdx.x, lane = tid & 63, w = __builtin_amdgcn_readfirstlane(tid >> 6);
  const int xcd = blockIdx.x & 7, bi = blockIdx.x >> 3;
  const int j = lane & 15, g = lane >> 4;
  const int P = 2 * xcd + (bi >> 4), c = bi & 15;
  const rsrc_t rw = make_rsrc(ws, WS_USED);
  ST_DECL; ST(0);
  u32x4 yv[2];
#pragma unroll
  for (int i = 0; i < 2; ++i) yv[i] = ld16<0>(rw, (tid >> 1) * COLB + (tid & 1) * 16, OFF_Y + i * 128 * COLB + (P * 32 + 2 * c) * 16);
  const int r0 = 2 * (w & 1), b0 = 2 * c + (w >> 1);
  u32x2 xv[2][2];
#pragma unroll
  for (int r = 0; r < 2; ++r)
#pragma unroll
    for (int at = 0; at < 2; ++at)
      xv[r][at] = *(const u32x2*)(x + (unsigned)((128 * P + 32 * (r0 + r) + b0) * D + 256 * at) + 16 * j + 4 * g);
  const unsigned hsoff = OFF_H + (8 * P + 2 * w) * 57344 + 7 * (c >> 1) * 1024;
#pragma unroll
  for (int m = 0; m < 2; ++m) {
#pragma unroll
    for (int u = 0; u < 3; ++u) st16<16>((u32x4){MARK, MARK, MARK, MARK}, rw, lane * 16 + u * 1024, hsoff + m * 57344 + (c & 1) * 3072);
    st8<16>((u32x2){MARK, MARK}, rw, lane * 16, hsoff + m * 57344 + 6144 + (c & 1) * 8);
  }
  u32x4 BR, BI, SR, SI, C2[2];
  f32x4 tc, ts;
  constexpr float SC = 1.f / 1048576.f;
#pragma unroll
  for (int h = 0; h < 4; ++h) {
    const int k2 = 4 * g + h;
    const float th = (float)((j * k2) & 15) * (1.f / 16.f);
    const float ct = cosr(th) * SC, st = sinr(th) * SC;
    const float ph = (float)((j * (g < 2 ? 2 * k2 : 2 * k2 - 15)) & 31) * (1.f / 32.f);
    const float cp = cosr(ph) * (2.f * SC), sp = sinr(ph) * (-2.f * SC);
    BR[h] = pk(ct, g < 2 ? -st : st);
    BI[h] = pk(st, g < 2 ? ct : -ct);
    const unsigned sv = k2 == 0 ? pk(SC, (j & 1) ? -SC : SC) : pk(cp, sp);
    SR[h] = g < 2 ? sv : 0u;
    SI[h] = g < 2 ? 0u : sv;
    const float tw = (float)(j * k2) * (1.f / 512.f);
    tc[h] = cosr(tw);
    ts[h] = sinr(tw);
    const float an = (float)((j * k2) & 31) * (1.f / 32.f);
    C2[0][h] = k2 == 0 ? pk(1.f, (j & 1) ? -1.f : 1.f) : pk(2.f * cosr(an), -2.f * sinr(an));
    C2[1][h] = C2[0][h] ^ ((h & 1) ? SS : 0u);
  }
  int yc[4];
#pragma unroll
  for (int h = 0; h < 4; ++h) { const int k = kcol(j, 4 * g + h); yc[h] = L_Y + k * 32 + (k >> 2) * 8 + (w >> 1) * 16 + r0 * 4; }
  ST(1);
#pragma unroll
  for (int i = 0; i < 2; ++i) {
    const int k = (tid >> 1) + 128 * i;
    char* p = lds + L_Y + k * 32 + (k >> 2) * 8 + (tid & 1) * 16;
    *(u32x2*)p = (u32x2){yv[i][0], yv[i][1]};
    *(u32x2*)(p + 8) = (u32x2){yv[i][2], yv[i][3]};
  }
  asm volatile("" : "+v"(xv[0][0]), "+v"(xv[0][1]), "+v"(xv[1][0]), "+v"(xv[1][1]));
  {
    const unsigned lb = (unsigned)(size_t)(LDSP char*)lds + L_W1 + w * 28672;
    const char* gb = ws + OFF_W1 + c * 114688 + w * 28672;
#pragma unroll
    for (int i = 0; i < 7; ++i)
      asm volatile("s_mov_b32 m0, %0\n\ts_nop 0\n\tglobal_load_lds_dwordx4 %1, %2\n\tglobal_load_lds_dwordx4 %1, %2 offset:1024\n\t"
                   "global_load_lds_dwordx4 %1, %2 offset:2048\n\tglobal_load_lds_dwordx4 %1, %2 offset:3072"
                   :: "s"(lb + i * 4096), "v"(lane * 16), "s"(gb + i * 4096) : "memory");
  }
  BAR();
  ST(2);
  const f32x4 z = {0.f, 0.f, 0.f, 0.f};
  {
    u32x2 ld[4];
#pragma unroll
    for (int h = 0; h < 4; ++h) ld[h] = *(const u32x2*)(lds + yc[h]);
    u32x4 hv[2];
#pragma unroll
    for (int r = 0; r < 2; ++r) {
      const u32x4 a = {ld[0][r], ld[1][r], ld[2][r], ld[3][r]};
      f32x4 ur = mfma(a, BR, z), ui = mfma(a, BI, z);
      const f32x4 sr = mfma(a, SR, z), si = mfma(a, SI, z);
      if (g == 0) { ur[0] = sr[0]; ui[0] = si[0]; }
      const u32x4 t = pk4(ur * tc - ui * ts, ui * tc + ur * ts);
#pragma unroll
      for (int at = 0; at < 2; ++at) {
        const f32x4 y = mfma(t, C2[at], z);
        const unsigned y0 = pk(y[0], y[1]), y1 = pk(y[2], y[3]);
        hv[r][2 * at] = pk(bflo(xv[r][at][0]) + bflo(y0), bfhi(xv[r][at][0]) + bfhi(y0));
        hv[r][2 * at + 1] = pk(bflo(xv[r][at][1]) + bflo(y1), bfhi(xv[r][at][1]) + bfhi(y1));
      }
    }
#pragma unroll
    for (int r = 0; r < 2; ++r) *(u32x4*)(lds + L_U + lane * 128 + ((2 * w + r) ^ (lane & 7)) * 16) = layernorm8(hv[r]);
    asm volatile("s_waitcnt vmcnt(28)" ::: "memory");
    BAR();
#pragma unroll
    for (int i = 0; i < 2; ++i) {
      const int o = (tid >> 3) + 32 * i;
      const u32x4 u = *(const u32x4*)(lds + L_U + o * 128 + ((tid & 7) ^ (o & 7)) * 16);
      st16<16>(u, rw, (o >> 2) * 1024 + (o & 3) * 256 + (tid & 7) * 16, OFF_A1 + (8 * P + (c >> 1)) * 16384 + (c & 1) * 128);
    }
#pragma unroll
    for (int r = 0; r < 2; ++r) st16<16>(hv[r], rw, lane * 16, OFF_HR + (128 * P + 8 * c + 2 * w + r) * 1024);
  }
  ST(3);
  asm volatile("s_waitcnt vmcnt(0)" ::: "memory");
  BAR();
  ST(4);
  const int rb = 4 * xcd + (bi >> 3), db = bi & 7;
  u32x4 wb[14][4];
  {
    const unsigned asoff = OFF_A1 + (8 * P + 2 * w) * 16384;
    u32x4 a[16][2];
    for (int sp = 0; sp < SPIN_MAX; ++sp) {
      asm volatile("" ::: "memory");
      unsigned mk = 0;
#pragma unroll
      for (int ks = 0; ks < 4; ++ks)
#pragma unroll
        for (int m = 0; m < 2; ++m) { a[ks][m] = ld16<16>(rw, lane * 16 + ks * 1024, asoff + m * 16384); }
#pragma unroll
      for (int ks = 0; ks < 4; ++ks)
#pragma unroll
        for (int m = 0; m < 2; ++m) mk = mark4(mk, a[ks][m]);
      if (!__any(mk == MARK)) break;
    }
    ST(5);
    f32x4 acc[2][7];
    for (int sp = 0; sp < SPIN_MAX; ++sp) {
      asm volatile("" ::: "memory");
#pragma unroll
      for (int ks = 4; ks < 16; ++ks)
#pragma unroll
        for (int m = 0; m < 2; ++m) a[ks][m] = ld16<16>(rw, lane * 16 + (ks & 3) * 1024, asoff + m * 16384 + (ks >> 2) * 4096);
      u32x4 bf[2][7];
#pragma unroll
      for (int t = 0; t < 7; ++t) bf[0][t] = *(const u32x4*)(lds + L_W1 + t * 1024 + lane * 16);
      unsigned mk = 0;
      SCHED();
#pragma unroll
      for (int ks = 0; ks < 16; ++ks) {
        if (ks >= 4) mk = mark4(mark4(mk, a[ks][0]), a[ks][1]);
#pragma unroll
        for (int t = 0; t < 7; ++t) {
          if (ks < 15) bf[(ks + 1) & 1][t] = *(const u32x4*)(lds + L_W1 + ((ks + 1) * 7 + t) * 1024 + lane * 16);
          if (ks >= 2 && t < 4) wb[ks - 2][t] = ld16<0>(rw, lane * 16 + t * 1024, OFF_W2 + (db * 56 + 14 * w + ks - 2) * 4096);
#pragma unroll
          for (int m = 0; m < 2; ++m) acc[m][t] = mfma(bf[ks & 1][t], a[ks][m], ks == 0 ? z : acc[m][t]);
          SCHED();
        }
      }
      if (!__any(mk == MARK)) break;
    }
    ST(6);
#pragma unroll
    for (int m = 0; m < 2; ++m) {
      u32x2 p[7];
#pragma unroll
      for (int t = 0; t < 7; ++t) {
        const f32x4 s = silu4(acc[m][t]);
        p[t] = (u32x2){pk(s[0], s[1]), pk(s[2], s[3])};
      }
#pragma unroll
      for (int u = 0; u < 3; ++u)
        st16<16>((u32x4){p[2 * u][0], p[2 * u][1], p[2 * u + 1][0], p[2 * u + 1][1]}, rw, lane * 16 + u * 1024, hsoff + m * 57344 + (c & 1) * 3072);
      st8<16>(p[6], rw, lane * 16, hsoff + m * 57344 + 6144 + (c & 1) * 8);
    }
  }
  ST(7);
  {
    const unsigned hoff = OFF_H + (4 * rb) * 57344 + 14 * w * 1024;
    const unsigned poff = ((lane >> 2) & 3) * 57344 + (7 * ((lane >> 1) & 1) + 6) * 1024 + lane * 16 + (lane & 1) * 8;
    u32x4 a[14][4];
    for (int sp = 0; sp < SPIN_MAX; ++sp) {
      asm volatile("" ::: "memory");
      unsigned mk = __builtin_amdgcn_raw_buffer_load_b32(rw, poff, hoff, 16);
#pragma unroll
      for (int ks = 0; ks < 2; ++ks)
#pragma unroll
        for (int m = 0; m < 4; ++m) a[ks][m] = ld16<16>(rw, lane * 16 + ks * 1024, hoff + m * 57344);
#pragma unroll
      for (int ks = 0; ks < 2; ++ks)
#pragma unroll
        for (int m = 0; m < 4; ++m) mk = mark4(mk, a[ks][m]);
      if (!__any(mk == MARK)) break;
    }
    ST(8);
    const int rt = 4 * rb + w;
    u32x2 hr[4];
    f32x4 acc[4][4];
    for (int sp = 0; sp < SPIN_MAX; ++sp) {
      asm volatile("" ::: "memory");
#pragma unroll
      for (int ks = 2; ks < 9; ++ks)
#pragma unroll
        for (int m = 0; m < 4; ++m) a[ks][m] = ld16<16>(rw, lane * 16 + (ks & 3) * 1024, hoff + m * 57344 + (ks >> 2) * 4096);
      unsigned mk = 0;
      SCHED();
#pragma unroll
      for (int ks = 0; ks < 14; ++ks) {
#pragma unroll
        for (int m = 0; m < 4; ++m) {
          if (ks >= 2) mk = mark4(mk, a[ks][m]);
#pragma unroll
          for (int t = 0; t < 4; ++t) acc[m][t] = mfma(wb[ks][t], a[ks][m], ks == 0 ? z : acc[m][t]);
          if (ks >= 2 && ks + 7 < 14) a[ks + 7][m] = ld16<16>(rw, lane * 16 + ((ks + 7) & 3) * 1024, hoff + m * 57344 + ((ks + 7) >> 2) * 4096);
          if (ks == 7) hr[m] = ld8<16>(rw, ((4 * (db & 3) + g) + 16 * m) * 16 + (db >> 2) * 8 + j * 1024, OFF_HR + rt * 16384);
          SCHED();
        }
      }
      if (!__any(mk == MARK)) break;
    }
    ST(9);
    BAR();
#pragma unroll
    for (int m = 0; m < 4; ++m)
#pragma unroll
      for (int t = 0; t < 4; ++t)
        if (m != w) *(f32x4*)(lds + (((w * 4 + m) * 4 + t) * 64 + lane) * 16) = acc[m][t];
    BAR();
    ST(10);
    unsigned o[8];
#pragma unroll
    for (int t = 0; t < 4; ++t) {
      f32x4 s = w == 0 ? acc[0][t] : w == 1 ? acc[1][t] : w == 2 ? acc[2][t] : acc[3][t];
#pragma unroll
      for (int v = 1; v < 4; ++v) s += *(const f32x4*)(lds + (((((w + v) & 3) * 4 + w) * 4 + t) * 64 + lane) * 16);
      const unsigned s0 = pk(s[0], s[1]), s1 = pk(s[2], s[3]);
      o[2 * t] = pk(bflo(hr[t][0]) + bflo(s0), bfhi(hr[t][0]) + bfhi(s0));
      o[2 * t + 1] = pk(bflo(hr[t][1]) + bflo(s1), bfhi(hr[t][1]) + bfhi(s1));
    }
    const int srow = 128 * (rt >> 3) + 32 * (j & 3) + 4 * (rt & 7) + (j >> 2);
    u16* op = out + (unsigned)(srow * D + 64 * db + 16 * g);
    *(u32x4*)op = (u32x4){o[0], o[1], o[2], o[3]};
    *(u32x4*)(op + 8) = (u32x4){o[4], o[5], o[6], o[7]};
  }
  ST(11);
  ST_END(2);
}

extern "C" void kernel_launch(void* const* d_in, const int* in_sizes, int n_in, void* d_out, int out_size, void* d_ws,
                              size_t ws_size, hipStream_t stream) {
  const u16* x = (const u16*)d_in[0];
  const u16* wr = (const u16*)d_in[1];
  const u16* wi = (const u16*)d_in[2];
  const u16* w1 = (const u16*)d_in[4];
  const u16* w2 = (const u16*)d_in[5];
  char* ws = (char*)d_ws;
  u16* out = (u16*)d_out;
  k1_ln_rfft<<<K1_FFT + K1_WT, 512, 0, stream>>>(x, wr, wi, ws);
  k2_colfft<<<K2_FFT + K2_W1 + K2_W2, 256, 0, stream>>>(w1, w2, ws);
  k345<<<256, 256, 0, stream>>>(x, ws, out);
}
```

```cpp
#include <hip/hip_runtime.h>
#include <hip/hip_bf16.h>

typedef __bf16 bf16x8 __attribute__((ext_vector_type(8)));
typedef __bf16 bf16x2 __attribute__((ext_vector_type(2)));
typedef float f32x2 __attribute__((ext_vector_type(2)));
typedef float f32x4 __attribute__((ext_vector_type(4)));
typedef short s16x4 __attribute__((ext_vector_type(4)));
typedef unsigned u32x2 __attribute__((ext_vector_type(2)));
typedef unsigned u32x2u __attribute__((ext_vector_type(2), aligned(2)));
typedef unsigned u32x4 __attribute__((ext_vector_type(4)));
typedef unsigned short u16;
typedef __amdgpu_buffer_rsrc_t rsrc_t;
#define DI __device__ __forceinline__
#define LDSP __attribute__((address_space(3)))
#define SCHED() __builtin_amdgcn_sched_barrier(0)
#define BAR() asm volatile("s_waitcnt lgkmcnt(0)\n\ts_barrier" ::: "memory")

constexpr int S = 2048, D = 512, DF = 257, F = 1792;
constexpr unsigned COLB = 8192;
constexpr unsigned OFF_Z = 0;
constexpr unsigned OFF_W = OFF_Z + 256 * COLB;
constexpr unsigned OFF_Y = OFF_W + 260 * COLB;
constexpr unsigned OFF_A1 = OFF_Y + 256 * COLB;
constexpr unsigned OFF_HR = OFF_A1 + 2097152;
constexpr unsigned OFF_W1 = OFF_HR + 2097152;
constexpr unsigned OFF_W2 = OFF_W1 + 1835008;
constexpr unsigned OFF_H = OFF_W2 + 1835008;
constexpr unsigned WS_USED = OFF_H + 7340032;
constexpr unsigned MARK = 0xffffffffu;
constexpr unsigned SS = 0x80008000u;
constexpr int SPIN_MAX = 1 << 17;

#define ST_DECL
#define ST(n)
#define ST_END(kid)

DI unsigned pk(float lo, float hi) {
  f32x2 f = {lo, hi};
  return __builtin_bit_cast(unsigned, __builtin_convertvector(f, bf16x2));
}
DI float bflo(unsigned v) { return __builtin_bit_cast(float, v << 16); }
DI float bfhi(unsigned v) { return __builtin_bit_cast(float, v & 0xffff0000u); }
DI float rbf(float x) { return bflo(pk(x, x)); }
DI f32x4 mfma(u32x4 a, u32x4 b, f32x4 c) {
  return __builtin_amdgcn_mfma_f32_16x16x32_bf16(__builtin_bit_cast(bf16x8, a), __builtin_bit_cast(bf16x8, b), c, 0, 0, 0);
}
DI u32x4 pk4(f32x4 lo, f32x4 hi) { return (u32x4){pk(lo[0], hi[0]), pk(lo[1], hi[1]), pk(lo[2], hi[2]), pk(lo[3], hi[3])}; }
DI float cosr(float r) { return __builtin_amdgcn_cosf(r); }
DI float sinr(float r) { return __builtin_amdgcn_sinf(r); }
DI rsrc_t make_rsrc(const void* p, unsigned bytes) { return __builtin_amdgcn_make_buffer_rsrc((void*)p, 0, bytes, 0x00020000); }
template <int AUX> DI u32x4 ld16(rsrc_t r, unsigned voff, unsigned soff) { return __builtin_amdgcn_raw_buffer_load_b128(r, voff, soff, AUX); }
template <int AUX> DI u32x2 ld8(rsrc_t r, unsigned voff, unsigned soff) { return __builtin_amdgcn_raw_buffer_load_b64(r, voff, soff, AUX); }
template <int AUX> DI void st16(u32x4 v, rsrc_t r, unsigned voff, unsigned soff) { __builtin_amdgcn_raw_buffer_store_b128(v, r, voff + soff, 0, AUX); }
template <int AUX> DI void st8(u32x2 v, rsrc_t r, unsigned voff, unsigned soff) { __builtin_amdgcn_raw_buffer_store_b64(v, r, voff, soff, AUX); }
DI u32x2 trread(const char* p) { return __builtin_bit_cast(u32x2, __builtin_amdgcn_ds_read_tr16_b64_v4i16((LDSP s16x4*)p)); }
DI unsigned umax3(unsigned a, unsigned b, unsigned c) { return max(max(a, b), c); }
DI unsigned mark4(unsigned m, u32x4 v) { return umax3(umax3(m, v[0], v[1]), v[2], v[3]); }

template <int CTRL>
DI float dpp_add(float v) {
  return v + __builtin_bit_cast(float, __builtin_amdgcn_update_dpp(0, __builtin_bit_cast(int, v), CTRL, 0xf, 0xf, true));
}
DI float wave_sum(float v) {
  v = dpp_add<0xB1>(v);
  v = dpp_add<0x4E>(v);
  v = dpp_add<0x141>(v);
  v = dpp_add<0x140>(v);
  const int iv = __builtin_bit_cast(int, v);
  const float a = __builtin_bit_cast(float, __builtin_amdgcn_readlane(iv, 0));
  const float b = __builtin_bit_cast(float, __builtin_amdgcn_readlane(iv, 16));
  const float c = __builtin_bit_cast(float, __builtin_amdgcn_readlane(iv, 32));
  const float d = __builtin_bit_cast(float, __builtin_amdgcn_readlane(iv, 48));
  return (a + b) + (c + d);
}
DI u32x4 layernorm8(u32x4 p) {
  const f32x4 lo = {bflo(p[0]), bflo(p[1]), bflo(p[2]), bflo(p[3])}, hi = {bfhi(p[0]), bfhi(p[1]), bfhi(p[2]), bfhi(p[3])};
  const f32x4 s4 = lo + hi, q4 = lo * lo + hi * hi;
  const float s = wave_sum((s4[0] + s4[1]) + (s4[2] + s4[3])), q = wave_sum((q4[0] + q4[1]) + (q4[2] + q4[3]));
  const float mean = s * (1.f / 512.f);
  const float var = fmaxf(q * (1.f / 512.f) - mean * mean, 0.f);
  const float rs = __builtin_amdgcn_rcpf(rbf(__builtin_amdgcn_sqrtf(rbf(rbf(var) + 1e-5f))));
  const float nb = -rbf(mean) * rs;
  return pk4(lo * rs + nb, hi * rs + nb);
}
DI int kcol(int k1, int k2) {
  if (k1 == 0) return k2 < 8 ? 32 * k2 : 32 * k2 - 240;
  return k2 < 8 ? k1 + 32 * k2 : 512 - k1 - 32 * k2;
}

constexpr int K1_FFT = 256, K1_WT = 64;
extern "C" __global__ __launch_bounds__(512, 2) void k1_ln_rfft(const u16* __restrict__ x, const u16* __restrict__ wr,
                                                     const u16* __restrict__ wi, char* __restrict__ ws) {
  __shared__ __attribute__((aligned(16))) char lds[24576];
  const int lane = threadIdx.x & 63, v = __builtin_amdgcn_readfirstlane(threadIdx.x >> 6);
  const int xcd = blockIdx.x & 7;
  const int j = lane & 15, g = lane >> 4;
  const rsrc_t rw = make_rsrc(ws, WS_USED);
  ST_DECL; ST(0);
  if (blockIdx.x < K1_FFT) {
    const int bi = blockIdx.x >> 3, P = 2 * xcd + (bi >> 4), c = bi & 15;
    const u32x4 xv = *(const u32x4*)(x + (unsigned)((128 * P + 32 * (v & 3) + 2 * c + (v >> 2)) * D) + 8 * lane);
    st16<0>((u32x4){MARK, MARK, MARK, MARK}, rw, threadIdx.x * 16, OFF_A1 + blockIdx.x * 8192);
    if (v == 7) {
      const char *c0, *c1;
      asm volatile("s_getpc_b64 s[98:99]\n\ts_add_u32 s98, s98, k2_colfft@rel32@lo+4\n\ts_addc_u32 s99, s99, k2_colfft@rel32@hi+12\n\ts_mov_b64 %0, s[98:99]\n\t"
                   "s_getpc_b64 s[98:99]\n\ts_add_u32 s98, s98, zz_end@rel32@lo+4\n\ts_addc_u32 s99, s99, zz_end@rel32@hi+12\n\ts_mov_b64 %1, s[98:99]"
                   : "=s"(c0), "=s"(c1) :: "s98", "s99", "scc");
      const char* p = c0 + bi * 1024 + lane * 16;
      if (p + 16 <= c1) { u32x4 t = *(const u32x4*)p; asm volatile("" :: "v"(t)); }
    }
    char* ct = lds + 8192;
    {
      u32x4 o;
#pragma unroll
      for (int h = 0; h < 4; ++h) {
        const int b = 4 * g + h;
        if (v < 2) {
          const int a0 = 4 * g + ((2 * h) & 3) + 16 * (h >> 1);
          const int jj = (v == 1 && j == 0) ? 16 : j;
          const float sh = (v == 1 && j != 0) ? 0.25f : 0.f;
          o[h] = pk(cosr((float)((a0 * jj) & 31) * (1.f / 32.f) + sh), cosr((float)(((a0 + 1) * jj) & 31) * (1.f / 32.f) + sh));
        } else if (v < 4) {
          const float th = (float)((b * j) & 15) * (1.f / 16.f);
          const float cs = cosr(th), sn = sinr(th);
          o[h] = v == 2 ? pk(cs, sn) : (pk(-sn, cs) ^ (j < 8 ? 0u : SS));
        } else if (v < 6) {
          const int m = j < 8 ? ((v == 5 && j == 0) ? 16 : 2 * j) : 2 * j - 15;
          const float val = cosr((float)((b * m) & 31) * (1.f / 32.f) + ((v == 5 && j != 0) ? 0.25f : 0.f));
          o[h] = j < 8 ? pk(val, 0.f) : pk(0.f, val);
        } else {
          o[h] = __builtin_bit_cast(unsigned, cosr((float)(b * j) * (1.f / 512.f) - (v == 7 ? 0.25f : 0.f)));
        }
      }
      *(u32x4*)(ct + v * 1024 + lane * 16) = o;
    }
    BAR();
    const u32x4 BR = *(const u32x4*)(ct + lane * 16), BI = *(const u32x4*)(ct + 1024 + lane * 16);
    const u32x4 AR = *(const u32x4*)(ct + 2048 + lane * 16), AI = *(const u32x4*)(ct + 3072 + lane * 16);
    const u32x4 SR = *(const u32x4*)(ct + 4096 + lane * 16), SI = *(const u32x4*)(ct + 5120 + lane * 16);
    const f32x4 tc = *(const f32x4*)(ct + 6144 + lane * 16), ts = *(const f32x4*)(ct + 7168 + lane * 16);
    char* so = lds + 16384 + v * 1024;
    int oc[4];
#pragma unroll
    for (int h = 0; h < 4; ++h) oc[h] = kcol(j, 4 * g + h) * 4;
    ST(1);
    char* wl = lds + v * 1024;
    *(u32x4*)(wl + lane * 16) = layernorm8(xv);
    ST(2);
    const u32x2 a0 = trread(wl + lane * 8), a1 = trread(wl + 512 + lane * 8);
    const u32x4 a = {a0[0], a0[1], a1[0], a1[1]};
    const f32x4 z = {0.f, 0.f, 0.f, 0.f};
    const f32x4 dr = mfma(a, BR, z), di = mfma(a, BI, z);
    const u32x4 t = pk4(dr * tc + di * ts, di * tc - dr * ts);
    const f32x4 xr = mfma(AR, t, z), xi = mfma(AI, t, z), sr = mfma(SR, t, z), si = mfma(SI, t, z);
    const u32x4 o1 = pk4(xr, xi), o2 = pk4(sr, si);
#pragma unroll
    for (int h = 0; h < 4; ++h) *(unsigned*)(so + oc[h]) = j == 0 ? o2[h] : o1[h];
    ST(3);
    BAR();
    const char* si_ = lds + 16384 + (threadIdx.x & 1) * 4096 + (threadIdx.x >> 1) * 4;
    const u32x4 o = {*(const unsigned*)si_, *(const unsigned*)(si_ + 1024), *(const unsigned*)(si_ + 2048), *(const unsigned*)(si_ + 3072)};
    st16<0>(o, rw, (threadIdx.x >> 1) * COLB + (threadIdx.x & 1) * 16, OFF_Z + (P * 32 + 2 * c) * 16);
    ST(4);
  } else {
    const int c = 64 * xcd + 8 * ((blockIdx.x - K1_FFT) >> 3) + v;
    const int p0 = (c & 63) + 256 * (c >> 6);
    u32x2 a[4], b[4];
#pragma unroll
    for (int r = 0; r < 4; ++r) {
      const unsigned ro = (unsigned)(p0 + 64 * r) * DF;
      a[r] = *(const u32x2u*)(wr + ro + 4 * lane);
      b[r] = *(const u32x2u*)(wi + ro + 4 * lane);
    }
    const int pe = p0 + 64 * (lane & 3), pm = (S - pe) & (S - 1);
    const float ar = bflo(wr[pe * DF]), ai = bflo(wi[pe * DF]), br = bflo(wr[pe * DF + 256]), bi = bflo(wi[pe * DF + 256]);
    const float cr = bflo(wr[pm * DF]), ci = bflo(wi[pm * DF]), dr = bflo(wr[pm * DF + 256]), di = bflo(wi[pm * DF + 256]);
#pragma unroll
    for (int q = 0; q < 4; ++q) {
      u32x4 o;
#pragma unroll
      for (int r = 0; r < 4; ++r)
        o[r] = __builtin_amdgcn_perm(b[r][q >> 1], a[r][q >> 1], (q & 1) ? 0x07060302u : 0x05040100u);
      if (q > 0 || lane > 0) st16<0>(o, rw, (4 * lane + q) * COLB, OFF_W + c * 16);
    }
    if (lane < 4) {
      const float Ar = ar + cr, Ai = ai - ci, Br = br + dr, Bi = bi - di;
      *(unsigned*)(ws + OFF_W + c * 16 + lane * 4) = pk(0.25f * (Ar + Br), 0.25f * (Ai + Bi));
      *(unsigned*)(ws + OFF_W + 256 * COLB + c * 16 + lane * 4) = pk(0.25f * (Ar - Br), 0.25f * (Ai - Bi));
    }
  }
  ST_END(0);
}

constexpr int K2_FFT = 256, K2_W1 = 64, K2_W2 = 112;
extern "C" __global__ __launch_bounds__(256, 2) void k2_colfft(const u16* __restrict__ w1, const u16* __restrict__ w2, char* __restrict__ ws) {
  __shared__ __attribute__((aligned(16))) char lds[28672];
  const int lane = threadIdx.x & 63, w = __builtin_amdgcn_readfirstlane(threadIdx.x >> 6);
  const int j = lane & 15, g = lane >> 4;
  const rsrc_t rw = make_rsrc(ws, WS_USED);
  ST_DECL; ST(0);
  if (blockIdx.x >= K2_FFT + K2_W1) {
    const int u = (blockIdx.x - K2_FFT - K2_W1) * 4 + w, db = u & 7, ks = u >> 3;
    const int q = ks / 7, jj = ks - 7 * q;
    const int flo = jj < 6 ? 224 * q + 112 * (jj >= 3) + 32 * (jj % 3) : 224 * q + 96, fd = jj < 6 ? 16 : 112;
    char* wl = lds + w * 4096;
    u32x4 t[4];
#pragma unroll
    for (int i = 0; i < 4; ++i) {
      const int r = 8 * (i & 1) + (lane >> 3);
      t[i] = *(const u32x4*)(w2 + (unsigned)((flo + (i >> 1) * fd + r) * D + 64 * db + 8 * (lane & 7)));
    }
#pragma unroll
    for (int i = 0; i < 4; ++i) *(u32x4*)(wl + i * 1024 + lane * 16) = t[i];
    const char* rp = wl + (4 * g + (j >> 2)) * 128 + (j & 3) * 32;
#pragma unroll
    for (int dt = 0; dt < 4; ++dt) {
      const u32x2 lo = trread(rp + dt * 8), hi = trread(rp + 2048 + dt * 8);
      st16<0>((u32x4){lo[0], lo[1], hi[0], hi[1]}, rw, lane * 16 + dt * 1024, OFF_W2 + (db * 56 + ks) * 4096);
    }
    ST_END(1);
    return;
  }
  if (blockIdx.x >= K2_FFT) {
    const int u = (blockIdx.x - K2_FFT) * 4 + w, cb = u & 15, ks = u >> 4;
    char* wl = lds + w * 7168;
    u32x4 t[7];
#pragma unroll
    for (int i = 0; i < 7; ++i) {
      const int e = i * 64 + lane, r = e / 14, ch = e - 14 * r;
      const int row = 64 * (ks & 3) + 4 * (ks >> 2) + 16 * ((r >> 2) & 3) + (r & 3) + 256 * (r >> 4);
      t[i] = *(const u32x4*)(w1 + (unsigned)(row * F + 112 * cb + 8 * ch));
    }
#pragma unroll
    for (int i = 0; i < 7; ++i) *(u32x4*)(wl + i * 1024 + lane * 16) = t[i];
    const char* rp = wl + (4 * g + (j >> 2)) * 224 + (j & 3) * 8;
#pragma unroll
    for (int tt = 0; tt < 7; ++tt) {
      const u32x2 lo = trread(rp + tt * 32), hi = trread(rp + 3584 + tt * 32);
      st16<0>((u32x4){lo[0], lo[1], hi[0], hi[1]}, rw, lane * 16 + tt * 1024, OFF_W1 + (cb * 16 + ks) * 7168);
    }
    ST_END(1);
    return;
  }
  const int col = blockIdx.x;
  u32x4 za[4][2];
#pragma unroll
  for (int t = 0; t < 4; ++t)
#pragma unroll
    for (int bt = 0; bt < 2; ++bt) za[t][bt] = ld16<0>(rw, (g * 32 + j) * 16 + t * 2048 + bt * 256, OFF_Z + col * COLB);
  u32x4 wv[2], wv2[2];
#pragma unroll
  for (int kt = 0; kt < 2; ++kt) wv[kt] = ld16<0>(rw, (g * 64 + j) * 16, OFF_W + col * COLB + kt * 4096 + w * 256);
  if (col == 0) {
#pragma unroll
    for (int kt = 0; kt < 2; ++kt) wv2[kt] = ld16<0>(rw, (g * 64 + j) * 16, OFF_W + 256 * COLB + kt * 4096 + w * 256);
  }
  u32x4 F1R[4], F1I[4], F2R[2][2], F2I[2][2];
  f32x4 fc[2], fs[2], ic[2], is[2];
  {
    const int k1 = 16 * w + j;
    const unsigned m1 = (j & 1) ? SS : 0u, mr = (j & 2) ? SS : 0u, mi = ((j + 1) & 2) ? SS : 0u;
#pragma unroll
    for (int h = 0; h < 4; ++h) {
      const float ang = (float)(((4 * g + h) * k1) & 63) * (1.f / 64.f);
      const float c = cosr(ang), s = sinr(ang);
      const unsigned r0 = pk(c, s), i0 = pk(-s, c);
      F1R[0][h] = r0; F1I[0][h] = i0;
      F1R[2][h] = r0 ^ m1; F1I[2][h] = i0 ^ m1;
      F1R[1][h] = ((j & 1) ? i0 : r0) ^ mr; F1I[1][h] = ((j & 1) ? r0 : i0) ^ mi;
      F1R[3][h] = F1R[1][h] ^ m1; F1I[3][h] = F1I[1][h] ^ m1;
      const float an2 = (float)(((4 * g + h) * j) & 31) * (1.f / 32.f);
      const float c2 = cosr(an2), s2 = sinr(an2);
      const unsigned r2 = pk(c2, s2), i2 = pk(-s2, c2), mh = (h & 1) ? SS : 0u;
      F2R[0][0][h] = r2; F2I[0][0][h] = i2;
      F2R[0][1][h] = r2 ^ m1; F2I[0][1][h] = i2 ^ m1;
      F2R[1][0][h] = r2 ^ mh; F2I[1][0][h] = i2 ^ mh;
      F2R[1][1][h] = r2 ^ m1 ^ mh; F2I[1][1][h] = i2 ^ m1 ^ mh;
#pragma unroll
      for (int bt = 0; bt < 2; ++bt) {
        const float a1 = (float)((16 * bt + 4 * g + h) * k1) * (1.f / 2048.f);
        fc[bt][h] = cosr(a1);
        fs[bt][h] = sinr(a1);
        const float a2 = (float)((16 * bt + j) * (16 * w + 4 * g + h)) * (1.f / 2048.f);
        ic[bt][h] = cosr(a2);
        is[bt][h] = sinr(a2);
      }
    }
  }
  ST(1);
  const f32x4 z = {0.f, 0.f, 0.f, 0.f};
  f32x4 re[2] = {z, z}, im[2] = {z, z};
#pragma unroll
  for (int t = 0; t < 4; ++t)
#pragma unroll
    for (int bt = 0; bt < 2; ++bt) { re[bt] = mfma(za[t][bt], F1R[t], re[bt]); im[bt] = mfma(za[t][bt], F1I[t], im[bt]); }
  u32x4 tf[2];
#pragma unroll
  for (int bt = 0; bt < 2; ++bt) tf[bt] = pk4(re[bt] * fc[bt] + im[bt] * fs[bt], im[bt] * fc[bt] - re[bt] * fs[bt]);
  ST(2);
#pragma unroll
  for (int kt = 0; kt < 2; ++kt) {
    re[kt] = mfma(F2R[kt][1], tf[1], mfma(F2R[kt][0], tf[0], z));
    im[kt] = mfma(F2I[kt][1], tf[1], mfma(F2I[kt][0], tf[0], z));
  }
  u32x4 yf[2];
  f32x4 yr[2], yi[2];
#pragma unroll
  for (int kt = 0; kt < 2; ++kt) {
    const f32x4 a = {bflo(wv[kt][0]), bflo(wv[kt][1]), bflo(wv[kt][2]), bflo(wv[kt][3])};
    const f32x4 b = {bfhi(wv[kt][0]), bfhi(wv[kt][1]), bfhi(wv[kt][2]), bfhi(wv[kt][3])};
    yr[kt] = re[kt] * a - im[kt] * b;
    yi[kt] = re[kt] * b + im[kt] * a;
  }
  if (col == 0) {
    char* ex = lds + 8192;
    const int p0 = 16 * w + j + 256 * g;
#pragma unroll
    for (int kt = 0; kt < 2; ++kt)
#pragma unroll
      for (int h = 0; h < 4; ++h) *(f32x2*)(ex + p0 * 8 + (16 * kt + h) * 512) = (f32x2){re[kt][h], im[kt][h]};
    if (threadIdx.x == 0) *(f32x2*)(ex + 2048 * 8) = (f32x2){re[0][0], im[0][0]};
    BAR();
#pragma unroll
    for (int kt = 0; kt < 2; ++kt)
#pragma unroll
      for (int h = 0; h < 4; ++h) {
        const f32x2 m = *(const f32x2*)(ex + (2048 - 19 * 64 - p0) * 8 + (19 - 16 * kt - h) * 512);
        const float a = bflo(wv2[kt][h]), b = bfhi(wv2[kt][h]);
        yr[kt][h] += m[0] * a + m[1] * b;
        yi[kt][h] += m[0] * b - m[1] * a;
      }
  }
#pragma unroll
  for (int kt = 0; kt < 2; ++kt) yf[kt] = pk4(yi[kt], yr[kt]);
#pragma unroll
  for (int bt = 0; bt < 2; ++bt) {
    im[bt] = mfma(yf[1], F2R[bt][1], mfma(yf[0], F2R[bt][0], z));
    re[bt] = mfma(yf[1], F2I[bt][1], mfma(yf[0], F2I[bt][0], z));
  }
#pragma unroll
  for (int bt = 0; bt < 2; ++bt)
    *(u32x4*)(lds + ((w * 2 + bt) * 64 + lane) * 16) = pk4(im[bt] * ic[bt] + re[bt] * is[bt], re[bt] * ic[bt] - im[bt] * is[bt]);
  ST(3);
  BAR();
  ST(4);
  u32x4 u[4][2];
#pragma unroll
  for (int t = 0; t < 4; ++t)
#pragma unroll
    for (int bt = 0; bt < 2; ++bt) u[t][bt] = *(const u32x4*)(lds + ((t * 2 + bt) * 64 + lane) * 16);
  re[0] = re[1] = im[0] = im[1] = z;
#pragma unroll
  for (int t = 0; t < 4; ++t)
#pragma unroll
    for (int bt = 0; bt < 2; ++bt) { im[bt] = mfma(F1R[t], u[t][bt], im[bt]); re[bt] = mfma(F1I[t], u[t][bt], re[bt]); }
#pragma unroll
  for (int bt = 0; bt < 2; ++bt) st16<0>(pk4(re[bt], im[bt]), rw, (g * 32 + j) * 16 + bt * 256, OFF_Y + col * COLB + w * 2048);
  ST(5);
  ST_END(1);
}

DI f32x4 silu4(f32x4 h) {
  const f32x4 t = h * -1.44269504088896f;
  const f32x4 e = {__builtin_amdgcn_exp2f(t[0]), __builtin_amdgcn_exp2f(t[1]), __builtin_amdgcn_exp2f(t[2]), __builtin_amdgcn_exp2f(t[3])};
  const f32x4 d = e + 1.f;
  const f32x4 r = {__builtin_amdgcn_rcpf(d[0]), __builtin_amdgcn_rcpf(d[1]), __builtin_amdgcn_rcpf(d[2]), __builtin_amdgcn_rcpf(d[3])};
  return h * r;
}

constexpr int L_W1 = 0, L_Y = 114688, L_U = L_Y + 8704, L_END = L_U + 8192;
extern "C" __global__ __launch_bounds__(256, 1) void k345(const u16* __restrict__ x, char* __restrict__ ws, u16* __restrict__ out) {
  __shared__ __attribute__((aligned(16))) char lds[L_END];
  const int tid = threadIdx.x, lane = tid & 63, w = __builtin_amdgcn_readfirstlane(tid >> 6);
  const int xcd = blockIdx.x & 7, bi = blockIdx.x >> 3;
  const int j = lane & 15, g = lane >> 4;
  const int P = 2 * xcd + (bi >> 4), c = bi & 15;
  const rsrc_t rw = make_rsrc(ws, WS_USED);
  ST_DECL; ST(0);
  u32x4 yv[2];
#pragma unroll
  for (int i = 0; i < 2; ++i) yv[i] = ld16<0>(rw, (tid >> 1) * COLB + (tid & 1) * 16, OFF_Y + i * 128 * COLB + (P * 32 + 2 * c) * 16);
  const int r0 = 2 * (w & 1), b0 = 2 * c + (w >> 1);
  u32x2 xv[2][2];
#pragma unroll
  for (int r = 0; r < 2; ++r)
#pragma unroll
    for (int at = 0; at < 2; ++at)
      xv[r][at] = *(const u32x2*)(x + (unsigned)((128 * P + 32 * (r0 + r) + b0) * D + 256 * at) + 16 * j + 4 * g);
  const unsigned hsoff = OFF_H + (8 * P + 2 * w) * 57344 + 7 * (c >> 1) * 1024;
#pragma unroll
  for (int m = 0; m < 2; ++m) {
#pragma unroll
    for (int u = 0; u < 3; ++u) st16<16>((u32x4){MARK, MARK, MARK, MARK}, rw, lane * 16 + u * 1024, hsoff + m * 57344 + (c & 1) * 3072);
    st8<16>((u32x2){MARK, MARK}, rw, lane * 16, hsoff + m * 57344 + 6144 + (c & 1) * 8);
  }
  u32x4 BR, BI, SR, SI, C2[2];
  f32x4 tc, ts;
  constexpr float SC = 1.f / 1048576.f;
#pragma unroll
  for (int h = 0; h < 4; ++h) {
    const int k2 = 4 * g + h;
    const float th = (float)((j * k2) & 15) * (1.f / 16.f);
    const float ct = cosr(th) * SC, st = sinr(th) * SC;
    const float ph = (float)((j * (g < 2 ? 2 * k2 : 2 * k2 - 15)) & 31) * (1.f / 32.f);
    const float cp = cosr(ph) * (2.f * SC), sp = sinr(ph) * (-2.f * SC);
    BR[h] = pk(ct, g < 2 ? -st : st);
    BI[h] = pk(st, g < 2 ? ct : -ct);
    const unsigned sv = k2 == 0 ? pk(SC, (j & 1) ? -SC : SC) : pk(cp, sp);
    SR[h] = g < 2 ? sv : 0u;
    SI[h] = g < 2 ? 0u : sv;
    const float tw = (float)(j * k2) * (1.f / 512.f);
    tc[h] = cosr(tw);
    ts[h] = sinr(tw);
    const float an = (float)((j * k2) & 31) * (1.f / 32.f);
    C2[0][h] = k2 == 0 ? pk(1.f, (j & 1) ? -1.f : 1.f) : pk(2.f * cosr(an), -2.f * sinr(an));
    C2[1][h] = C2[0][h] ^ ((h & 1) ? SS : 0u);
  }
  int yc[4];
#pragma unroll
  for (int h = 0; h < 4; ++h) { const int k = kcol(j, 4 * g + h); yc[h] = L_Y + k * 32 + (k >> 2) * 8 + (w >> 1) * 16 + r0 * 4; }
  ST(1);
#pragma unroll
  for (int i = 0; i < 2; ++i) {
    const int k = (tid >> 1) + 128 * i;
    char* p = lds + L_Y + k * 32 + (k >> 2) * 8 + (tid & 1) * 16;
    *(u32x2*)p = (u32x2){yv[i][0], yv[i][1]};
    *(u32x2*)(p + 8) = (u32x2){yv[i][2], yv[i][3]};
  }
  asm volatile("" : "+v"(xv[0][0]), "+v"(xv[0][1]), "+v"(xv[1][0]), "+v"(xv[1][1]));
  {
    const unsigned lb = (unsigned)(size_t)(LDSP char*)lds + L_W1 + w * 28672;
    const char* gb = ws + OFF_W1 + c * 114688 + w * 28672;
#pragma unroll
    for (int i = 0; i < 7; ++i)
      asm volatile("s_mov_b32 m0, %0\n\ts_nop 0\n\tglobal_load_lds_dwordx4 %1, %2\n\tglobal_load_lds_dwordx4 %1, %2 offset:1024\n\t"
                   "global_load_lds_dwordx4 %1, %2 offset:2048\n\tglobal_load_lds_dwordx4 %1, %2 offset:3072"
                   :: "s"(lb + i * 4096), "v"(lane * 16), "s"(gb + i * 4096) : "memory");
  }
  BAR();
  ST(2);
  const f32x4 z = {0.f, 0.f, 0.f, 0.f};
  {
    u32x2 ld[4];
#pragma unroll
    for (int h = 0; h < 4; ++h) ld[h] = *(const u32x2*)(lds + yc[h]);
    u32x4 hv[2];
#pragma unroll
    for (int r = 0; r < 2; ++r) {
      const u32x4 a = {ld[0][r], ld[1][r], ld[2][r], ld[3][r]};
      f32x4 ur = mfma(a, BR, z), ui = mfma(a, BI, z);
      const f32x4 sr = mfma(a, SR, z), si = mfma(a, SI, z);
      if (g == 0) { ur[0] = sr[0]; ui[0] = si[0]; }
      const u32x4 t = pk4(ur * tc - ui * ts, ui * tc + ur * ts);
#pragma unroll
      for (int at = 0; at < 2; ++at) {
        const f32x4 y = mfma(t, C2[at], z);
        const unsigned y0 = pk(y[0], y[1]), y1 = pk(y[2], y[3]);
        hv[r][2 * at] = pk(bflo(xv[r][at][0]) + bflo(y0), bfhi(xv[r][at][0]) + bfhi(y0));
        hv[r][2 * at + 1] = pk(bflo(xv[r][at][1]) + bflo(y1), bfhi(xv[r][at][1]) + bfhi(y1));
      }
    }
#pragma unroll
    for (int r = 0; r < 2; ++r) *(u32x4*)(lds + L_U + lane * 128 + ((2 * w + r) ^ (lane & 7)) * 16) = layernorm8(hv[r]);
    asm volatile("s_waitcnt vmcnt(28)" ::: "memory");
    BAR();
#pragma unroll
    for (int i = 0; i < 2; ++i) {
      const int o = (tid >> 3) + 32 * i;
      const u32x4 u = *(const u32x4*)(lds + L_U + o * 128 + ((tid & 7) ^ (o & 7)) * 16);
      st16<16>(u, rw, (o >> 2) * 1024 + (o & 3) * 256 + (tid & 7) * 16, OFF_A1 + (8 * P + (c >> 1)) * 16384 + (c & 1) * 128);
    }
#pragma unroll
    for (int r = 0; r < 2; ++r) st16<16>(hv[r], rw, lane * 16, OFF_HR + (128 * P + 8 * c + 2 * w + r) * 1024);
  }
  ST(3);
  asm volatile("s_waitcnt vmcnt(0)" ::: "memory");
  BAR();
  ST(4);
  const int rb = 4 * xcd + (bi >> 3), db = bi & 7;
  u32x4 wb[14][4];
  {
    const unsigned asoff = OFF_A1 + (8 * P + 2 * w) * 16384;
    u32x4 a[16][2];
    for (int sp = 0; sp < SPIN_MAX; ++sp) {
      asm volatile("" ::: "memory");
      unsigned mk = 0;
#pragma unroll
      for (int ks = 0; ks < 4; ++ks)
#pragma unroll
        for (int m = 0; m < 2; ++m) { a[ks][m] = ld16<16>(rw, lane * 16 + ks * 1024, asoff + m * 16384); }
#pragma unroll
      for (int ks = 0; ks < 4; ++ks)
#pragma unroll
        for (int m = 0; m < 2; ++m) mk = mark4(mk, a[ks][m]);
      if (!__any(mk == MARK)) break;
    }
    ST(5);
    f32x4 acc[2][7];
    for (int sp = 0; sp < SPIN_MAX; ++sp) {
      asm volatile("" ::: "memory");
#pragma unroll
      for (int ks = 4; ks < 16; ++ks)
#pragma unroll
        for (int m = 0; m < 2; ++m) a[ks][m] = ld16<16>(rw, lane * 16 + (ks & 3) * 1024, asoff + m * 16384 + (ks >> 2) * 4096);
      u32x4 bf[2][7];
#pragma unroll
      for (int t = 0; t < 7; ++t) bf[0][t] = *(const u32x4*)(lds + L_W1 + t * 1024 + lane * 16);
      unsigned mk = 0;
      SCHED();
#pragma unroll
      for (int ks = 0; ks < 16; ++ks) {
        if (ks >= 4) mk = mark4(mark4(mk, a[ks][0]), a[ks][1]);
#pragma unroll
        for (int t = 0; t < 7; ++t) {
          if (ks < 15) bf[(ks + 1) & 1][t] = *(const u32x4*)(lds + L_W1 + ((ks + 1) * 7 + t) * 1024 + lane * 16);
          if (ks >= 2 && t < 4) wb[ks - 2][t] = ld16<0>(rw, lane * 16 + t * 1024, OFF_W2 + (db * 56 + 14 * w + ks - 2) * 4096);
#pragma unroll
          for (int m = 0; m < 2; ++m) acc[m][t] = mfma(bf[ks & 1][t], a[ks][m], ks == 0 ? z : acc[m][t]);
          SCHED();
        }
      }
      if (!__any(mk == MARK)) break;
    }
    ST(6);
#pragma unroll
    for (int m = 0; m < 2; ++m) {
      u32x2 p[7];
#pragma unroll
      for (int t = 0; t < 7; ++t) {
        const f32x4 s = silu4(acc[m][t]);
        p[t] = (u32x2){pk(s[0], s[1]), pk(s[2], s[3])};
      }
#pragma unroll
      for (int u = 0; u < 3; ++u)
        st16<16>((u32x4){p[2 * u][0], p[2 * u][1], p[2 * u + 1][0], p[2 * u + 1][1]}, rw, lane * 16 + u * 1024, hsoff + m * 57344 + (c & 1) * 3072);
      st8<16>(p[6], rw, lane * 16, hsoff + m * 57344 + 6144 + (c & 1) * 8);
    }
  }
  ST(7);
  {
    const unsigned hoff = OFF_H + (4 * rb) * 57344 + 14 * w * 1024;
    const unsigned poff = ((lane >> 2) & 3) * 57344 + (7 * ((lane >> 1) & 1) + 6) * 1024 + lane * 16 + (lane & 1) * 8;
    u32x4 a[14][4];
    for (int sp = 0; sp < SPIN_MAX; ++sp) {
      asm volatile("" ::: "memory");
      unsigned mk = __builtin_amdgcn_raw_buffer_load_b32(rw, poff, hoff, 16);
#pragma unroll
      for (int ks = 0; ks < 2; ++ks)
#pragma unroll
        for (int m = 0; m < 4; ++m) a[ks][m] = ld16<16>(rw, lane * 16 + ks * 1024, hoff + m * 57344);
#pragma unroll
      for (int ks = 0; ks < 2; ++ks)
#pragma unroll
        for (int m = 0; m < 4; ++m) mk = mark4(mk, a[ks][m]);
      if (!__any(mk == MARK)) break;
    }
    ST(8);
    const int rt = 4 * rb + w;
    u32x2 hr[4];
    f32x4 acc[4][4];
    for (int sp = 0; sp < SPIN_MAX; ++sp) {
      asm volatile("" ::: "memory");
#pragma unroll
      for (int ks = 2; ks < 9; ++ks)
#pragma unroll
        for (int m = 0; m < 4; ++m) a[ks][m] = ld16<16>(rw, lane * 16 + (ks & 3) * 1024, hoff + m * 57344 + (ks >> 2) * 4096);
      unsigned mk = 0;
      SCHED();
#pragma unroll
      for (int ks = 0; ks < 14; ++ks) {
#pragma unroll
        for (int m = 0; m < 4; ++m) {
          if (ks >= 2) mk = mark4(mk, a[ks][m]);
#pragma unroll
          for (int t = 0; t < 4; ++t) acc[m][t] = mfma(wb[ks][t], a[ks][m], ks == 0 ? z : acc[m][t]);
          if (ks >= 2 && ks + 7 < 14) a[ks + 7][m] = ld16<16>(rw, lane * 16 + ((ks + 7) & 3) * 1024, hoff + m * 57344 + ((ks + 7) >> 2) * 4096);
          if (ks == 7) hr[m] = ld8<16>(rw, ((4 * (db & 3) + g) + 16 * m) * 16 + (db >> 2) * 8 + j * 1024, OFF_HR + rt * 16384);
          SCHED();
        }
      }
      if (!__any(mk == MARK)) break;
    }
    ST(9);
    BAR();
#pragma unroll
    for (int m = 0; m < 4; ++m)
#pragma unroll
      for (int t = 0; t < 4; ++t)
        if (m != w) *(f32x4*)(lds + (((w * 4 + m) * 4 + t) * 64 + lane) * 16) = acc[m][t];
    BAR();
    ST(10);
    unsigned o[8];
#pragma unroll
    for (int t = 0; t < 4; ++t) {
      f32x4 s = w == 0 ? acc[0][t] : w == 1 ? acc[1][t] : w == 2 ? acc[2][t] : acc[3][t];
#pragma unroll
      for (int v = 1; v < 4; ++v) s += *(const f32x4*)(lds + (((((w + v) & 3) * 4 + w) * 4 + t) * 64 + lane) * 16);
      const unsigned s0 = pk(s[0], s[1]), s1 = pk(s[2], s[3]);
      o[2 * t] = pk(bflo(hr[t][0]) + bflo(s0), bfhi(hr[t][0]) + bfhi(s0));
      o[2 * t + 1] = pk(bflo(hr[t][1]) + bflo(s1), bfhi(hr[t][1]) + bfhi(s1));
    }
    const int srow = 128 * (rt >> 3) + 32 * (j & 3) + 4 * (rt & 7) + (j >> 2);
    u16* op = out + (unsigned)(srow * D + 64 * db + 16 * g);
    *(u32x4*)op = (u32x4){o[0], o[1], o[2], o[3]};
    *(u32x4*)(op + 8) = (u32x4){o[4], o[5], o[6], o[7]};
  }
  ST(11);
  ST_END(2);
}

extern "C" __global__ void zz_end(char* ws) { if (ws == nullptr) ws[0] = 1; }

extern "C" void kernel_launch(void* const* d_in, const int* in_sizes, int n_in, void* d_out, int out_size, void* d_ws,
                              size_t ws_size, hipStream_t stream) {
  const u16* x = (const u16*)d_in[0];
  const u16* wr = (const u16*)d_in[1];
  const u16* wi = (const u16*)d_in[2];
  const u16* w1 = (const u16*)d_in[4];
  const u16* w2 = (const u16*)d_in[5];
  char* ws = (char*)d_ws;
  u16* out = (u16*)d_out;
  k1_ln_rfft<<<K1_FFT + K1_WT, 512, 0, stream>>>(x, wr, wi, ws);
  k2_colfft<<<K2_FFT + K2_W1 + K2_W2, 256, 0, stream>>>(w1, w2, ws);
  k345<<<256, 256, 0, stream>>>(x, ws, out);
}
```

```cpp
#include <hip/hip_runtime.h>
#include <hip/hip_bf16.h>

typedef __bf16 bf16x8 __attribute__((ext_vector_type(8)));
typedef __bf16 bf16x2 __attribute__((ext_vector_type(2)));
typedef float f32x2 __attribute__((ext_vector_type(2)));
typedef float f32x4 __attribute__((ext_vector_type(4)));
typedef short s16x4 __attribute__((ext_vector_type(4)));
typedef unsigned u32x2 __attribute__((ext_vector_type(2)));
typedef unsigned u32x2u __attribute__((ext_vector_type(2), aligned(2)));
typedef unsigned u32x4 __attribute__((ext_vector_type(4)));
typedef unsigned short u16;
#define DI __device__ __forceinline__
#define LDSP __attribute__((address_space(3)))

constexpr int S = 2048, D = 512, DF = 257, F = 1792;
constexpr size_t COLB = 8192;
constexpr size_t OFF_Z = 0;
constexpr size_t OFF_W = OFF_Z + 260 * COLB;
constexpr size_t OFF_Y = OFF_W + 260 * COLB;
constexpr size_t OFF_A1 = OFF_Y + 260 * COLB;
constexpr size_t OFF_W1 = OFF_A1 + 2097152;
constexpr size_t OFF_W2 = OFF_W1 + 1835008;
constexpr size_t OFF_H = OFF_W2 + 1835008;

#define ST_DECL
#define ST(n)
#define ST_END(kid)

DI unsigned pk(float lo, float hi) {
  f32x2 f = {lo, hi};
  return __builtin_bit_cast(unsigned, __builtin_convertvector(f, bf16x2));
}
DI float bflo(unsigned v) { return __builtin_bit_cast(float, v << 16); }
DI float bfhi(unsigned v) { return __builtin_bit_cast(float, v & 0xffff0000u); }
DI float rbf(float x) { return bflo(pk(x, x)); }
DI f32x4 mfma(u32x4 a, u32x4 b, f32x4 c) {
  return __builtin_amdgcn_mfma_f32_16x16x32_bf16(__builtin_bit_cast(bf16x8, a), __builtin_bit_cast(bf16x8, b), c, 0, 0, 0);
}
DI float cosr(float r) { return __builtin_amdgcn_cosf(r); }
DI float sinr(float r) { return __builtin_amdgcn_sinf(r); }
template <int CTRL, int RM>
DI float dpp_add(float v) {
  return v + __builtin_bit_cast(float, __builtin_amdgcn_update_dpp(0, __builtin_bit_cast(int, v), CTRL, RM, 0xf, true));
}
DI float wave_sum(float v) {
  v = dpp_add<0xB1, 0xf>(v);
  v = dpp_add<0x4E, 0xf>(v);
  v = dpp_add<0x141, 0xf>(v);
  v = dpp_add<0x140, 0xf>(v);
  const int iv = __builtin_bit_cast(int, v);
  const float a = __builtin_bit_cast(float, __builtin_amdgcn_readlane(iv, 0));
  const float b = __builtin_bit_cast(float, __builtin_amdgcn_readlane(iv, 16));
  const float c = __builtin_bit_cast(float, __builtin_amdgcn_readlane(iv, 32));
  const float d = __builtin_bit_cast(float, __builtin_amdgcn_readlane(iv, 48));
  return (a + b) + (c + d);
}
DI void layernorm8(const float (&v)[8], float (&o)[8]) {
  float s = ((v[0] + v[1]) + (v[2] + v[3])) + ((v[4] + v[5]) + (v[6] + v[7]));
  const float mean = wave_sum(s) * (1.f / 512.f);
  float q = 0.f;
#pragma unroll
  for (int e = 0; e < 8; ++e) { const float d = v[e] - mean; q += d * d; }
  const float var = wave_sum(q) * (1.f / 512.f);
  const float mb = rbf(mean);
  const float sd = rbf(__builtin_amdgcn_sqrtf(rbf(rbf(var) + 1e-5f)));
  const float rs = __builtin_amdgcn_rcpf(sd);
#pragma unroll
  for (int e = 0; e < 8; ++e) o[e] = rbf(v[e] - mb) * rs;
}
DI int kcol(int k1, int k2) {
  if (k1 == 0) return k2 < 8 ? 32 * k2 : 32 * k2 - 240;
  return k2 < 8 ? k1 + 32 * k2 : 512 - k1 - 32 * k2;
}

DI void k1_ln_rfft(int VB, char* lds, const u16*  x, const u16*  wr,
                                                  const u16*  wi, char*  ws) {
  const int lane = threadIdx.x & 63, wave = __builtin_amdgcn_readfirstlane(threadIdx.x >> 6);
  const int xcd = VB & 7, bi = VB >> 3;
  const int j = lane & 15, g = lane >> 4;
  ST_DECL; ST(0);
  if (wave < 2) {
    const int P = 2 * xcd + (bi >> 4), b0 = 2 * (bi & 15) + wave;
    u32x4 xv[4];
#pragma unroll
    for (int r = 0; r < 4; ++r) xv[r] = *(const u32x4*)(x + (size_t)(128 * P + 32 * r + b0) * D + 8 * lane);
    u32x4 BR, BI, AR, AI, SR, SI;
    float tc[4], ts[4];
#pragma unroll
    for (int h = 0; h < 4; ++h) {
      float c[2], s[2];
#pragma unroll
      for (int q = 0; q < 2; ++q) {
        const int e = 2 * h + q, a = 4 * g + (e & 3) + 16 * (e >> 2);
        const float ang = (float)((a * j) & 31) * (1.f / 32.f);
        c[q] = cosr(ang);
        s[q] = j == 0 ? ((a & 1) ? -1.f : 1.f) : -sinr(ang);
      }
      BR[h] = pk(c[0], c[1]);
      BI[h] = pk(s[0], s[1]);
      const int b = 4 * g + h;
      const float th = (float)((b * j) & 15) * (1.f / 16.f);
      const float ct = cosr(th), st = sinr(th);
      const float ph = (float)((b * (2 * j - 15)) & 31) * (1.f / 32.f);
      const float cp = cosr(ph), sp = sinr(ph);
      AR[h] = pk(ct, st);
      AI[h] = j < 8 ? pk(-st, ct) : pk(st, -ct);
      SR[h] = j < 8 ? pk(ct, 0.f) : pk(0.f, cp);
      SI[h] = j < 8 ? pk(j == 0 ? ((b & 1) ? -1.f : 1.f) : -st, 0.f) : pk(0.f, -sp);
      const float tw = (float)(b * j) * (1.f / 512.f);
      tc[h] = cosr(tw);
      ts[h] = sinr(tw);
    }
    ST(1);
    char* wl = lds + wave * 4096;
#pragma unroll
    for (int r = 0; r < 4; ++r) {
      float v[8], o[8];
#pragma unroll
      for (int h = 0; h < 4; ++h) { v[2 * h] = bflo(xv[r][h]); v[2 * h + 1] = bfhi(xv[r][h]); }
      layernorm8(v, o);
      u32x4 u;
#pragma unroll
      for (int h = 0; h < 4; ++h) u[h] = pk(o[2 * h], o[2 * h + 1]);
      *(u32x4*)(wl + r * 1024 + lane * 16) = u;
    }
    ST(2);
    u32x4 outv[4];
    unsigned nyq[4];
#pragma unroll
    for (int r = 0; r < 4; ++r) {
      const s16x4 t0 = __builtin_amdgcn_ds_read_tr16_b64_v4i16((LDSP s16x4*)(wl + r * 1024 + lane * 8));
      const s16x4 t1 = __builtin_amdgcn_ds_read_tr16_b64_v4i16((LDSP s16x4*)(wl + r * 1024 + 512 + lane * 8));
      const u32x2 a0 = __builtin_bit_cast(u32x2, t0), a1 = __builtin_bit_cast(u32x2, t1);
      const u32x4 a = {a0[0], a0[1], a1[0], a1[1]};
      const f32x4 z = {0.f, 0.f, 0.f, 0.f};
      const f32x4 dr = mfma(a, BR, z), di = mfma(a, BI, z);
      u32x4 t;
#pragma unroll
      for (int h = 0; h < 4; ++h) t[h] = pk(dr[h] * tc[h] + di[h] * ts[h], di[h] * tc[h] - dr[h] * ts[h]);
      const f32x4 xr = mfma(AR, t, z), xi = mfma(AI, t, z), sr = mfma(SR, t, z), si = mfma(SI, t, z);
#pragma unroll
      for (int h = 0; h < 4; ++h) outv[h][r] = j == 0 ? pk(sr[h], si[h]) : pk(xr[h], xi[h]);
      nyq[r] = outv[0][r] >> 16;
      if (lane == 0) outv[0][r] &= 0xffffu;
    }
    ST(3);
    char* zb = ws + OFF_Z + (size_t)(P * 32 + b0) * 16;
#pragma unroll
    for (int h = 0; h < 4; ++h) *(u32x4*)(zb + (size_t)kcol(j, 4 * g + h) * COLB) = outv[h];
    if (lane == 0) *(u32x4*)(zb + (size_t)256 * COLB) = (u32x4){nyq[0], nyq[1], nyq[2], nyq[3]};
  } else {
    const int c = 64 * xcd + 2 * bi + (wave - 2);
    const int p0 = (c & 63) + 256 * (c >> 6);
    u32x2 a[4], b[4];
    u16 ea[4], eb[4];
#pragma unroll
    for (int r = 0; r < 4; ++r) {
      const size_t ro = (size_t)(p0 + 64 * r) * DF;
      a[r] = *(const u32x2u*)(wr + ro + 4 * lane);
      b[r] = *(const u32x2u*)(wi + ro + 4 * lane);
      ea[r] = wr[ro + 256];
      eb[r] = wi[ro + 256];
    }
    char* wb = ws + OFF_W + (size_t)c * 16;
#pragma unroll
    for (int q = 0; q < 4; ++q) {
      u32x4 o;
#pragma unroll
      for (int r = 0; r < 4; ++r)
        o[r] = __builtin_amdgcn_perm(b[r][q >> 1], a[r][q >> 1], (q & 1) ? 0x07060302u : 0x05040100u);
      *(u32x4*)(wb + (size_t)(4 * lane + q) * COLB) = o;
    }
    if (lane == 0) {
      u32x4 o;
#pragma unroll
      for (int r = 0; r < 4; ++r) o[r] = (unsigned)ea[r] | ((unsigned)eb[r] << 16);
      *(u32x4*)(wb + (size_t)256 * COLB) = o;
    }
  }
  ST_END(0);
}

DI void retile_weights(const u16*  w1, const u16*  w2, char*  ws, int wg, int wave, int lane) {
  const int n = lane & 15, g = lane >> 4;
  if (wg < 128) {
    const int cb = wg >> 3, ks0 = 2 * (wg & 7);
    for (int f = wave; f < 14; f += 4) {
      const int ks = ks0 + (f >= 7), t = f >= 7 ? f - 7 : f;
      const int o = 4 * ks + g, n0 = 16 * (o & 15) + 4 * (o >> 4);
      const u16* src = w1 + (size_t)n0 * F + 112 * cb + 16 * t + n;
      u16 v[8];
#pragma unroll
      for (int e = 0; e < 8; ++e) v[e] = src[(size_t)((e & 3) + 256 * (e >> 2)) * F];
      u32x4 o4;
#pragma unroll
      for (int h = 0; h < 4; ++h) o4[h] = (unsigned)v[2 * h] | ((unsigned)v[2 * h + 1] << 16);
      *(u32x4*)(ws + OFF_W1 + ((size_t)((cb * 16 + ks) * 7 + t) * 64 + lane) * 16) = o4;
    }
  } else {
    const int f0 = (wg - 128) * 14;
    for (int f = wave; f < 14; f += 4) {
      const int fr = f0 + f, dt = fr & 3, ks = (fr >> 2) % 56, db = (fr >> 2) / 56;
      const int q = ks / 7, jj = ks % 7;
      const int d = 64 * db + 16 * (n >> 2) + 4 * dt + (n & 3);
      int flo, fhi;
      if (jj < 6) { flo = 112 * (2 * q + (jj >= 3)) + 32 * (jj % 3) + 4 * g; fhi = flo + 16; }
      else { flo = 224 * q + 96 + 4 * g; fhi = flo + 112; }
      u16 v[8];
#pragma unroll
      for (int e = 0; e < 4; ++e) { v[e] = w2[(size_t)(flo + e) * D + d]; v[4 + e] = w2[(size_t)(fhi + e) * D + d]; }
      u32x4 o4;
#pragma unroll
      for (int h = 0; h < 4; ++h) o4[h] = (unsigned)v[2 * h] | ((unsigned)v[2 * h + 1] << 16);
      *(u32x4*)(ws + OFF_W2 + ((size_t)fr * 64 + lane) * 16) = o4;
    }
  }
}

DI void k2_colfft(int VB, char* lds, const u16*  w1, const u16*  w2, char*  ws) {
  const int lane = threadIdx.x & 63, w = __builtin_amdgcn_readfirstlane(threadIdx.x >> 6);
  ST_DECL; ST(0);
  if (VB >= DF) { retile_weights(w1, w2, ws, VB - DF, w, lane); ST_END(1); return; }
  const int col = VB;
  const int j = lane & 15, g = lane >> 4;
  const char* zc = ws + OFF_Z + (size_t)col * COLB;
  u32x4 za[4][2];
#pragma unroll
  for (int t = 0; t < 4; ++t)
#pragma unroll
    for (int bt = 0; bt < 2; ++bt) za[t][bt] = *(const u32x4*)(zc + (((4 * t + g) * 32 + 16 * bt + j) * 16));
  const char* wc = ws + OFF_W + (size_t)col * COLB;
  u32x4 wv[2];
#pragma unroll
  for (int kt = 0; kt < 2; ++kt) wv[kt] = *(const u32x4*)(wc + (((4 * kt + g) * 64 + 16 * w + j) * 16));
  u32x4 F1R[4], F1I[4], F2R[2][2], F2I[2][2];
  float fc[2][4], fs[2][4], ic[2][4], is[2][4];
#pragma unroll
  for (int t = 0; t < 4; ++t)
#pragma unroll
    for (int h = 0; h < 4; ++h) {
      const float ang = (float)(((16 * t + 4 * g + h) * (16 * w + j)) & 63) * (1.f / 64.f);
      const float c = cosr(ang), s = sinr(ang);
      F1R[t][h] = pk(c, s);
      F1I[t][h] = pk(-s, c);
    }
#pragma unroll
  for (int kt = 0; kt < 2; ++kt)
#pragma unroll
    for (int bt = 0; bt < 2; ++bt)
#pragma unroll
      for (int h = 0; h < 4; ++h) {
        const float ang = (float)(((16 * bt + 4 * g + h) * (16 * kt + j)) & 31) * (1.f / 32.f);
        const float c = cosr(ang), s = sinr(ang);
        F2R[kt][bt][h] = pk(c, s);
        F2I[kt][bt][h] = pk(-s, c);
      }
#pragma unroll
  for (int bt = 0; bt < 2; ++bt)
#pragma unroll
    for (int h = 0; h < 4; ++h) {
      const float a1 = (float)((16 * bt + 4 * g + h) * (16 * w + j)) * (1.f / 2048.f);
      fc[bt][h] = cosr(a1);
      fs[bt][h] = sinr(a1);
      const float a2 = (float)((16 * bt + j) * (16 * w + 4 * g + h)) * (1.f / 2048.f);
      ic[bt][h] = cosr(a2) * (1.f / 1048576.f);
      is[bt][h] = sinr(a2) * (1.f / 1048576.f);
    }
  const f32x4 z = {0.f, 0.f, 0.f, 0.f};
  ST(1);
  u32x4 tf[2];
#pragma unroll
  for (int bt = 0; bt < 2; ++bt) {
    f32x4 re = z, im = z;
#pragma unroll
    for (int t = 0; t < 4; ++t) { re = mfma(za[t][bt], F1R[t], re); im = mfma(za[t][bt], F1I[t], im); }
#pragma unroll
    for (int h = 0; h < 4; ++h) tf[bt][h] = pk(re[h] * fc[bt][h] + im[h] * fs[bt][h], im[h] * fc[bt][h] - re[h] * fs[bt][h]);
  }
  ST(2);
  u32x4 yf[2];
#pragma unroll
  for (int kt = 0; kt < 2; ++kt) {
    f32x4 re = z, im = z;
#pragma unroll
    for (int bt = 0; bt < 2; ++bt) { re = mfma(F2R[kt][bt], tf[bt], re); im = mfma(F2I[kt][bt], tf[bt], im); }
#pragma unroll
    for (int h = 0; h < 4; ++h) {
      const float a = bflo(wv[kt][h]), b = bfhi(wv[kt][h]);
      yf[kt][h] = pk(re[h] * b + im[h] * a, re[h] * a - im[h] * b);
    }
  }
  u32x4 uf[2];
#pragma unroll
  for (int bt = 0; bt < 2; ++bt) {
    f32x4 re = z, im = z;
#pragma unroll
    for (int kt = 0; kt < 2; ++kt) { im = mfma(yf[kt], F2R[bt][kt], im); re = mfma(yf[kt], F2I[bt][kt], re); }
#pragma unroll
    for (int h = 0; h < 4; ++h) uf[bt][h] = pk(im[h] * ic[bt][h] + re[h] * is[bt][h], re[h] * ic[bt][h] - im[h] * is[bt][h]);
    *(u32x4*)(lds + ((w * 2 + bt) * 64 + lane) * 16) = uf[bt];
  }
  ST(3);
  __syncthreads();
  ST(4);
  char* yc = ws + OFF_Y + (size_t)col * COLB;
#pragma unroll
  for (int bt = 0; bt < 2; ++bt) {
    f32x4 re = z, im = z;
#pragma unroll
    for (int t = 0; t < 4; ++t) {
      const u32x4 u = *(const u32x4*)(lds + ((t * 2 + bt) * 64 + lane) * 16);
      im = mfma(F1R[t], u, im);
      re = mfma(F1I[t], u, re);
    }
    u32x4 o;
#pragma unroll
    for (int h = 0; h < 4; ++h) o[h] = pk(re[h], im[h]);
    *(u32x4*)(yc + (((4 * w + g) * 32 + 16 * bt + j) * 16)) = o;
  }
  ST(5);
  ST_END(1);
}

DI void k3_irfft_ln(int VB, char* lds, const u16*  x, char*  ws, u16*  out) {
  const int lane = threadIdx.x & 63, wave = __builtin_amdgcn_readfirstlane(threadIdx.x >> 6);
  const int xcd = VB & 7, bi = VB >> 3;
  const int j = lane & 15, g = lane >> 4;
  const int P = 2 * xcd + (bi >> 4), b0 = 2 * (bi & 15) + (wave >> 1), r0 = 2 * (wave & 1);
  ST_DECL; ST(0);
  const char* yb = ws + OFF_Y + (size_t)(P * 32 + b0) * 16 + r0 * 4;
  u32x2 ld[4];
#pragma unroll
  for (int h = 0; h < 4; ++h) ld[h] = *(const u32x2*)(yb + (size_t)kcol(j, 4 * g + h) * COLB);
  const u32x2 nyq = *(const u32x2*)(yb + (size_t)256 * COLB);
  u32x2 xv[2][2];
#pragma unroll
  for (int r = 0; r < 2; ++r)
#pragma unroll
    for (int at = 0; at < 2; ++at)
      xv[r][at] = *(const u32x2*)(x + (size_t)(128 * P + 32 * (r0 + r) + b0) * D + 256 * at + 16 * j + 4 * g);
  u32x4 BR, BI, SR, SI, C2[2];
  float tc[4], ts[4];
#pragma unroll
  for (int h = 0; h < 4; ++h) {
    const int k2 = 4 * g + h;
    const float th = (float)((j * k2) & 15) * (1.f / 16.f);
    const float ct = cosr(th), st = sinr(th);
    const float ph = (float)((j * (2 * k2 - 15)) & 31) * (1.f / 32.f);
    const float cp = cosr(ph), sp = sinr(ph);
    BR[h] = k2 < 8 ? pk(ct, -st) : pk(ct, st);
    BI[h] = k2 < 8 ? pk(st, ct) : pk(st, -ct);
    SR[h] = k2 == 0 ? pk(1.f, (j & 1) ? -1.f : 1.f) : (k2 < 8 ? pk(2.f * ct, -2.f * st) : 0u);
    SI[h] = k2 < 8 ? 0u : pk(2.f * cp, -2.f * sp);
    const float tw = (float)(j * k2) * (1.f / 512.f);
    tc[h] = cosr(tw);
    ts[h] = sinr(tw);
#pragma unroll
    for (int at = 0; at < 2; ++at) {
      const float an = (float)(((16 * at + j) * k2) & 31) * (1.f / 32.f);
      C2[at][h] = k2 == 0 ? pk(1.f, (j & 1) ? -1.f : 1.f) : pk(2.f * cosr(an), -2.f * sinr(an));
    }
  }
  ST(1);
  const f32x4 z = {0.f, 0.f, 0.f, 0.f};
#pragma unroll
  for (int r = 0; r < 2; ++r) {
    u32x4 a = {ld[0][r], ld[1][r], ld[2][r], ld[3][r]};
    if (lane == 0) a[0] = (a[0] & 0xffffu) | (nyq[r] << 16);
    f32x4 ur = mfma(a, BR, z), ui = mfma(a, BI, z);
    const f32x4 sr = mfma(a, SR, z), si = mfma(a, SI, z);
    if (g == 0) { ur[0] = sr[0]; ui[0] = si[0]; }
    u32x4 t;
#pragma unroll
    for (int h = 0; h < 4; ++h) t[h] = pk(ur[h] * tc[h] - ui[h] * ts[h], ui[h] * tc[h] + ur[h] * ts[h]);
    float v[8], o[8];
    u32x2 hv[2];
#pragma unroll
    for (int at = 0; at < 2; ++at) {
      const f32x4 y = mfma(t, C2[at], z);
      hv[at][0] = pk(bflo(xv[r][at][0]) + rbf(y[0]), bfhi(xv[r][at][0]) + rbf(y[1]));
      hv[at][1] = pk(bflo(xv[r][at][1]) + rbf(y[2]), bfhi(xv[r][at][1]) + rbf(y[3]));
      v[4 * at] = bflo(hv[at][0]); v[4 * at + 1] = bfhi(hv[at][0]);
      v[4 * at + 2] = bflo(hv[at][1]); v[4 * at + 3] = bfhi(hv[at][1]);
      *(u32x2*)(out + (size_t)(128 * P + 32 * (r0 + r) + b0) * D + 256 * at + 16 * j + 4 * g) = hv[at];
    }
    layernorm8(v, o);
    u32x4 u;
#pragma unroll
    for (int h = 0; h < 4; ++h) u[h] = pk(o[2 * h], o[2 * h + 1]);
    const int rho = 128 * P + 4 * b0 + r0 + r;
    *(u32x4*)(ws + OFF_A1 + ((size_t)(((rho >> 4) * 16 + (lane >> 2)) * 64 + (lane & 3) * 16 + (rho & 15))) * 16) = u;
  }
  ST(2);
  ST_END(2);
}

DI float silu_bf(float v) {
  const float h = rbf(v);
  return h * __builtin_amdgcn_rcpf(1.f + __builtin_amdgcn_exp2f(-1.44269504088896f * h));
}

DI void g1_gemm(int VB, char* lds, char*  ws) {
  const int lane = threadIdx.x & 63, w = __builtin_amdgcn_readfirstlane(threadIdx.x >> 6);
  const int xcd = VB & 7, bi = VB >> 3;
  const int P = 2 * xcd + (bi >> 4), cb = bi & 15;
  ST_DECL; ST(0);
  const char* wsrc = ws + OFF_W1 + (size_t)cb * 114688 + lane * 16;
#pragma unroll
  for (int i = 0; i < 28; ++i)
    __builtin_amdgcn_global_load_lds((const __attribute__((address_space(1))) void*)(wsrc + (i * 4 + w) * 1024),
                                     (LDSP void*)(lds + (i * 4 + w) * 1024), 16, 0, 0);
  const char* asrc = ws + OFF_A1 + (size_t)(8 * P + 2 * w) * 16384 + lane * 16;
  u32x4 a[2][16];
#pragma unroll
  for (int ks = 0; ks < 16; ++ks)
#pragma unroll
    for (int m = 0; m < 2; ++m) a[m][ks] = *(const u32x4*)(asrc + m * 16384 + ks * 1024);
  f32x4 acc[2][7];
#pragma unroll
  for (int m = 0; m < 2; ++m)
#pragma unroll
    for (int t = 0; t < 7; ++t) acc[m][t] = (f32x4){0.f, 0.f, 0.f, 0.f};
  ST(1);
  asm volatile("s_waitcnt vmcnt(0)" ::: "memory");
  ST(2);
  __syncthreads();
  ST(3);
#pragma unroll
  for (int ks = 0; ks < 16; ++ks)
#pragma unroll
    for (int t = 0; t < 7; ++t) {
      const u32x4 b = *(const u32x4*)(lds + (ks * 7 + t) * 1024 + lane * 16);
#pragma unroll
      for (int m = 0; m < 2; ++m) acc[m][t] = mfma(b, a[m][ks], acc[m][t]);
    }
  ST(4);
  const int ksb = 7 * (cb >> 1) + 3 * (cb & 1);
#pragma unroll
  for (int m = 0; m < 2; ++m) {
    char* hb = ws + OFF_H + (size_t)(8 * P + 2 * w + m) * 57344 + lane * 16;
    u32x2 p[7];
#pragma unroll
    for (int t = 0; t < 7; ++t) {
      p[t][0] = pk(silu_bf(acc[m][t][0]), silu_bf(acc[m][t][1]));
      p[t][1] = pk(silu_bf(acc[m][t][2]), silu_bf(acc[m][t][3]));
    }
#pragma unroll
    for (int u = 0; u < 3; ++u) *(u32x4*)(hb + (ksb + u) * 1024) = (u32x4){p[2 * u][0], p[2 * u][1], p[2 * u + 1][0], p[2 * u + 1][1]};
    *(u32x2*)(hb + (7 * (cb >> 1) + 6) * 1024 + (cb & 1) * 8) = p[6];
  }
  ST(5);
  ST_END(3);
}

DI void g2_gemm(int VB, char* lds, char*  ws, u16*  out) {
  const int lane = threadIdx.x & 63, w = __builtin_amdgcn_readfirstlane(threadIdx.x >> 6);
  const int xcd = VB & 7, bi = VB >> 3;
  const int rb = 4 * xcd + (bi >> 3), db = bi & 7;
  const int i = lane & 15, g = lane >> 4;
  ST_DECL; ST(0);
  const int rt = 4 * rb + w;
  const int srow = 128 * (rt >> 3) + 32 * (i & 3) + 4 * (rt & 7) + (i >> 2);
  u16* op = out + (size_t)srow * D + 64 * db + 16 * g;
  const u32x4 h0 = *(const u32x4*)op, h1 = *(const u32x4*)(op + 8);
  const char* bsrc = ws + OFF_W2 + ((size_t)db * 56 + 14 * w) * 4096 + lane * 16;
  const char* asrc = ws + OFF_H + (size_t)(4 * rb) * 57344 + (size_t)(14 * w) * 1024 + lane * 16;
  f32x4 acc[4][4];
#pragma unroll
  for (int m = 0; m < 4; ++m)
#pragma unroll
    for (int t = 0; t < 4; ++t) acc[m][t] = (f32x4){0.f, 0.f, 0.f, 0.f};
#pragma unroll
  for (int ks = 0; ks < 14; ++ks) {
    u32x4 a[4], b[4];
#pragma unroll
    for (int m = 0; m < 4; ++m) { a[m] = *(const u32x4*)(asrc + m * 57344 + ks * 1024); b[m] = *(const u32x4*)(bsrc + ks * 4096 + m * 1024); }
#pragma unroll
    for (int m = 0; m < 4; ++m)
#pragma unroll
      for (int t = 0; t < 4; ++t) acc[m][t] = mfma(b[t], a[m], acc[m][t]);
  }
  ST(1);
#pragma unroll
  for (int m = 0; m < 4; ++m)
#pragma unroll
    for (int t = 0; t < 4; ++t) *(f32x4*)(lds + (((w * 4 + m) * 4 + t) * 64 + lane) * 16) = acc[m][t];
  __syncthreads();
  ST(2);
  unsigned o[8];
#pragma unroll
  for (int t = 0; t < 4; ++t) {
    f32x4 s = *(const f32x4*)(lds + (((0 * 4 + w) * 4 + t) * 64 + lane) * 16);
#pragma unroll
    for (int v = 1; v < 4; ++v) s += *(const f32x4*)(lds + (((v * 4 + w) * 4 + t) * 64 + lane) * 16);
    const unsigned ha = t < 2 ? h0[2 * t] : h1[2 * t - 4], hb = t < 2 ? h0[2 * t + 1] : h1[2 * t - 3];
    o[2 * t] = pk(bflo(ha) + rbf(s[0]), bfhi(ha) + rbf(s[1]));
    o[2 * t + 1] = pk(bflo(hb) + rbf(s[2]), bfhi(hb) + rbf(s[3]));
  }
  *(u32x4*)op = (u32x4){o[0], o[1], o[2], o[3]};
  *(u32x4*)(op + 8) = (u32x4){o[4], o[5], o[6], o[7]};
  ST(3);
  ST_END(4);
}

typedef __attribute__((address_space(1))) unsigned long long gu64;
DI void gbar(char* ws, unsigned long long tag) {
  __syncthreads();
  gu64* fl = (gu64*)(ws + (40u << 20));
  if (threadIdx.x == 0) { __threadfence(); asm volatile("s_waitcnt vmcnt(0)" ::: "memory"); __hip_atomic_store(fl + blockIdx.x * 16, tag, __ATOMIC_RELAXED, __HIP_MEMORY_SCOPE_AGENT); }
  if (threadIdx.x < 64) {
    for (int sp = 0; sp < 200000; ++sp) {
      bool ok = true;
      for (int q = 0; q < 4; ++q) { const unsigned long long v = __hip_atomic_load(fl + (q * 64 + threadIdx.x) * 16, __ATOMIC_RELAXED, __HIP_MEMORY_SCOPE_AGENT); ok &= (v - tag) < 8; }
      if (__all(ok)) break;
    }
    __threadfence();
    asm volatile("s_waitcnt vmcnt(0)" ::: "memory");
  }
  __syncthreads();
}
__global__ __launch_bounds__(256, 1) void mega(const u16* x, const u16* wr, const u16* wi, const u16* w1, const u16* w2, char* ws, u16* out) {
  __shared__ __attribute__((aligned(16))) char lds[114688];
  const unsigned long long d0 = __builtin_amdgcn_s_memrealtime();
  gu64* ep = (gu64*)(ws + (39u << 20));
  const unsigned long long e = __hip_atomic_load(ep, __ATOMIC_RELAXED, __HIP_MEMORY_SCOPE_AGENT) + 16;
  k1_ln_rfft(blockIdx.x, lds, x, wr, wi, ws);
  gbar(ws, e);
  if (blockIdx.x == 0 && threadIdx.x == 0) __hip_atomic_store(ep, e, __ATOMIC_RELAXED, __HIP_MEMORY_SCOPE_AGENT);
  k2_colfft(blockIdx.x, lds, w1, w2, ws);
  __syncthreads();
  k2_colfft(blockIdx.x + 257, lds, w1, w2, ws);
  __syncthreads();
  if (blockIdx.x == 255) k2_colfft(256, lds, w1, w2, ws);
  gbar(ws, e + 1);
  k3_irfft_ln(blockIdx.x, lds, x, ws, out);
  gbar(ws, e + 2);
  g1_gemm(blockIdx.x, lds, ws);
  gbar(ws, e + 3);
  g2_gemm(blockIdx.x, lds, ws, out);
}
extern "C" void kernel_launch(void* const* d_in, const int* in_sizes, int n_in, void* d_out, int out_size, void* d_ws,
                              size_t ws_size, hipStream_t stream) {
  mega<<<256, 256, 0, stream>>>((const u16*)d_in[0], (const u16*)d_in[1], (const u16*)d_in[2], (const u16*)d_in[4], (const u16*)d_in[5], (char*)d_ws, (u16*)d_out);
}
```

```cpp
#include <hip/hip_runtime.h>
#include <hip/hip_bf16.h>

typedef __bf16 bf16x2 __attribute__((ext_vector_type(2)));
typedef float f32x2 __attribute__((ext_vector_type(2)));
typedef float f32x4 __attribute__((ext_vector_type(4)));
typedef short s16x4 __attribute__((ext_vector_type(4)));
typedef unsigned u32x2 __attribute__((ext_vector_type(2)));
typedef unsigned u32x2u __attribute__((ext_vector_type(2), aligned(2)));
typedef unsigned u32x4 __attribute__((ext_vector_type(4)));
typedef unsigned short u16;
typedef unsigned long long u64;
typedef __attribute__((address_space(1))) u64 gu64;
typedef __amdgpu_buffer_rsrc_t rsrc_t;
#define DI __device__ __forceinline__
#define LDSP __attribute__((address_space(3)))
#define SCHED() __builtin_amdgcn_sched_barrier(0)
#define BAR() asm volatile("s_waitcnt lgkmcnt(0)\n\ts_barrier" ::: "memory")
#define FENCE() asm volatile("" ::: "memory")

constexpr int S = 2048, D = 512, DF = 257, F = 1792;
constexpr unsigned COLB = 8192;
constexpr unsigned OFF_Z = 0;
constexpr unsigned OFF_W = OFF_Z + 256 * COLB;
constexpr unsigned OFF_Y = OFF_W + 260 * COLB;
constexpr unsigned OFF_A1 = OFF_Y + 256 * COLB;
constexpr unsigned OFF_RS = OFF_A1 + 2097152;
constexpr unsigned OFF_W1 = OFF_RS + 16384;
constexpr unsigned OFF_W2 = OFF_W1 + 1835008;
constexpr unsigned OFF_H = OFF_W2 + 1835008;
constexpr unsigned OFF_FL = OFF_H + 7340032;
constexpr unsigned OFF_FH = OFF_FL + 257 * 128;
constexpr unsigned WS_USED = OFF_FH + 2048 * 64;
constexpr unsigned MARK = 0xffffffffu;
constexpr unsigned SS = 0x80008000u;
constexpr int SPIN_MAX = 1 << 17;
constexpr int SC1 = 16;

DI unsigned pk(float lo, float hi) {
  f32x2 f = {lo, hi};
  return __builtin_bit_cast(unsigned, __builtin_convertvector(f, bf16x2));
}
DI float bflo(unsigned v) { return __builtin_bit_cast(float, v << 16); }
DI float bfhi(unsigned v) { return __builtin_bit_cast(float, v & 0xffff0000u); }
DI float rbf(float x) { return bflo(pk(x, x)); }
DI u32x4 pk4(f32x4 lo, f32x4 hi) { return (u32x4){pk(lo[0], hi[0]), pk(lo[1], hi[1]), pk(lo[2], hi[2]), pk(lo[3], hi[3])}; }
DI float cosr(float r) { return __builtin_amdgcn_cosf(r); }
DI float sinr(float r) { return __builtin_amdgcn_sinf(r); }
DI rsrc_t make_rsrc(const void* p, unsigned bytes) { return __builtin_amdgcn_make_buffer_rsrc((void*)p, 0, bytes, 0x00020000); }
DI u32x4 ld16(rsrc_t r, unsigned voff, unsigned soff) { return __builtin_amdgcn_raw_buffer_load_b128(r, voff, soff, SC1); }
DI u32x2 ld8(rsrc_t r, unsigned voff, unsigned soff) { return __builtin_amdgcn_raw_buffer_load_b64(r, voff, soff, SC1); }
DI unsigned ld4(rsrc_t r, unsigned voff, unsigned soff) { return __builtin_amdgcn_raw_buffer_load_b32(r, voff, soff, SC1); }
DI void st16(u32x4 v, rsrc_t r, unsigned voff, unsigned soff) { __builtin_amdgcn_raw_buffer_store_b128(v, r, voff + soff, 0, SC1); }
DI void st8(u32x2 v, rsrc_t r, unsigned voff, unsigned soff) { __builtin_amdgcn_raw_buffer_store_b64(v, r, voff, soff, SC1); }
DI void st4(unsigned v, rsrc_t r, unsigned voff, unsigned soff) { __builtin_amdgcn_raw_buffer_store_b32(v, r, voff, soff, SC1); }
DI u32x2 trread(const char* p) { return __builtin_bit_cast(u32x2, __builtin_amdgcn_ds_read_tr16_b64_v4i16((LDSP s16x4*)p)); }
DI unsigned umax3(unsigned a, unsigned b, unsigned c) { return max(max(a, b), c); }
DI unsigned mark4(unsigned m, u32x4 v) { return umax3(umax3(m, v[0], v[1]), v[2], v[3]); }

template <int CTRL>
DI float dpp_add(float v) {
  return v + __builtin_bit_cast(float, __builtin_amdgcn_update_dpp(0, __builtin_bit_cast(int, v), CTRL, 0xf, 0xf, true));
}
template <int CTRL>
DI float dpp_get(float v) { return __builtin_bit_cast(float, __builtin_amdgcn_update_dpp(0, __builtin_bit_cast(int, v), CTRL, 0xf, 0xf, true)); }
DI f32x4 unlo(u32x4 p) { return (f32x4){bflo(p[0]), bflo(p[1]), bflo(p[2]), bflo(p[3])}; }
DI f32x4 unhi(u32x4 p) { return (f32x4){bfhi(p[0]), bfhi(p[1]), bfhi(p[2]), bfhi(p[3])}; }
DI float hsum(f32x4 v) { const f32x2 t = __builtin_shufflevector(v, v, 0, 1) + __builtin_shufflevector(v, v, 2, 3); return t[0] + t[1]; }
DI void ln_sums2(const f32x4 (&v)[2][2], int lane, float& mean, float& var) {
  const float s0 = hsum(v[0][0] + v[0][1]), q0 = hsum(v[0][0] * v[0][0] + v[0][1] * v[0][1]);
  const float s1 = hsum(v[1][0] + v[1][1]), q1 = hsum(v[1][0] * v[1][0] + v[1][1] * v[1][1]);
  const bool o1 = lane & 1, o2 = lane & 2;
  const float a = (o1 ? q0 : s0) + dpp_get<0xB1>(o1 ? s0 : q0), b = (o1 ? q1 : s1) + dpp_get<0xB1>(o1 ? s1 : q1);
  float t = (o2 ? b : a) + dpp_get<0x4E>(o2 ? a : b);
  t = dpp_add<0x128>(dpp_add<0x124>(t));
  f32x4 sm;
  asm volatile("s_nop 1\n\tv_mfma_f32_16x16x4_f32 %0, %2, %1, 0\n\ts_nop 9" : "=&v"(sm) : "v"(t), "v"(1.f));
  mean = dpp_get<0xA0>(sm[0]) * (1.f / 512.f);
  var = dpp_get<0xF5>(sm[0]) * (1.f / 512.f) - mean * mean;
}
DI float ln_sd(float var) { return rbf(__builtin_amdgcn_sqrtf(rbf(rbf(fmaxf(var, 0.f)) + 1e-5f))); }
DI int kslot(int k) {
  const int r = k & 31, q = k >> 5;
  const int k1 = r < 16 ? r : (32 - r) & 15, k2 = r < 16 ? q : (r == 16 ? 8 + q : 15 - q);
  return k1 + 16 * (k2 >> 2) + 64 * (k2 & 3);
}
DI f32x4 silu4(f32x4 v, float sd, float c) {
  const f32x4 t = v * c;
  const f32x4 e = {__builtin_amdgcn_exp2f(t[0]), __builtin_amdgcn_exp2f(t[1]), __builtin_amdgcn_exp2f(t[2]), __builtin_amdgcn_exp2f(t[3])};
  const f32x4 d = e * sd + sd;
  const f32x4 r = {__builtin_amdgcn_rcpf(d[0]), __builtin_amdgcn_rcpf(d[1]), __builtin_amdgcn_rcpf(d[2]), __builtin_amdgcn_rcpf(d[3])};
  return v * r;
}

#define MF "v_mfma_f32_16x16x32_bf16 "
DI void mma0(f32x4& c, const u32x4& a, const u32x4& b) { asm volatile(MF "%0, %1, %2, 0" : "=&v"(c) : "v"(a), "v"(b)); }
DI void mma(f32x4& c, const u32x4& a, const u32x4& b) { asm volatile(MF "%0, %1, %2, %0" : "+v"(c) : "v"(a), "v"(b)); }
DI void mma0a(f32x4& c, const u32x4& a, const u32x4& b) { asm volatile(MF "%0, %1, %2, 0" : "=&v"(c) : "a"(a), "v"(b)); }
DI void mmaa(f32x4& c, const u32x4& a, const u32x4& b) { asm volatile(MF "%0, %1, %2, %0" : "+v"(c) : "a"(a), "v"(b)); }
#define PAD_IN2(a, b) asm volatile("s_nop 1" : "+v"(a), "+v"(b))
#define PAD_OUT4(a, b, c, d) asm volatile("s_nop 7" : "+v"(a), "+v"(b), "+v"(c), "+v"(d))

#define RELOAD16(F00, F01, F02, F03, F10, F11, F12, F13, F20, F21, F22, F23, F30, F31, F32, F33, S0, S1, S2, S3)        \
  asm volatile("buffer_load_dwordx4 %0, %17, %16, %18 offen sc1\n\tbuffer_load_dwordx4 %1, %17, %16, %19 offen sc1\n\t" \
               "buffer_load_dwordx4 %2, %17, %16, %20 offen sc1\n\tbuffer_load_dwordx4 %3, %17, %16, %21 offen sc1\n\t" \
               "buffer_load_dwordx4 %4, %17, %16, %18 offen offset:1024 sc1\n\tbuffer_load_dwordx4 %5, %17, %16, %19 offen offset:1024 sc1\n\t" \
               "buffer_load_dwordx4 %6, %17, %16, %20 offen offset:1024 sc1\n\tbuffer_load_dwordx4 %7, %17, %16, %21 offen offset:1024 sc1\n\t" \
               "buffer_load_dwordx4 %8, %17, %16, %18 offen offset:2048 sc1\n\tbuffer_load_dwordx4 %9, %17, %16, %19 offen offset:2048 sc1\n\t" \
               "buffer_load_dwordx4 %10, %17, %16, %20 offen offset:2048 sc1\n\tbuffer_load_dwordx4 %11, %17, %16, %21 offen offset:2048 sc1\n\t" \
               "buffer_load_dwordx4 %12, %17, %16, %18 offen offset:3072 sc1\n\tbuffer_load_dwordx4 %13, %17, %16, %19 offen offset:3072 sc1\n\t" \
               "buffer_load_dwordx4 %14, %17, %16, %20 offen offset:3072 sc1\n\tbuffer_load_dwordx4 %15, %17, %16, %21 offen offset:3072 sc1\n\t" \
               "s_waitcnt vmcnt(0)"                                                                                    \
               : "=&v"(F00), "=&v"(F01), "=&v"(F02), "=&v"(F03), "=&v"(F10), "=&v"(F11), "=&v"(F12), "=&v"(F13),       \
                 "=&v"(F20), "=&v"(F21), "=&v"(F22), "=&v"(F23), "=&v"(F30), "=&v"(F31), "=&v"(F32), "=&v"(F33)        \
               : "s"(rw), "v"(lane * 16), "s"(S0), "s"(S1), "s"(S2), "s"(S3) : "memory")
#define SPIN_FLAG(PV, PS) do {                                                                                         \
  u64 t_; unsigned n_;                                                                                                 \
  asm volatile("s_mov_b32 %1, 0x20000\n"                                                                               \
               "1:\n\t"                                                                                                \
               "buffer_load_dwordx2 %0, %3, %2, %4 offen sc1\n\t"                                                      \
               "s_waitcnt vmcnt(0)\n\t"                                                                                \
               "v_cmp_ne_u64 vcc, %0, %5\n\t"                                                                          \
               "s_sub_u32 %1, %1, 1\n\t"                                                                               \
               "s_cbranch_scc1 2f\n\t"                                                                                 \
               "s_and_b64 vcc, exec, vcc\n\t"                                                                          \
               "s_cbranch_vccnz 1b\n"                                                                                  \
               "2:"                                                                                                    \
               : "=&v"(t_), "=&s"(n_) : "s"(rw), "v"(PV), "s"(PS), "v"(tag) : "vcc", "scc", "memory"); } while (0)

#define PAGE(n) asm volatile("s_branch .Lpe" #n "\n\t.p2align 12\n.Lpg" #n ":\n\ts_setpc_b64 s[98:99]\n.Lpe" #n ":" ::: "memory")
#define WARM(n) asm volatile("s_getpc_b64 s[96:97]\n\ts_add_u32 s96, s96, .Lpg" #n "@rel32@lo+4\n\ts_addc_u32 s97, s97, .Lpg" #n "@rel32@hi+12\n\ts_swappc_b64 s[98:99], s[96:97]" \
                             ::: "s96", "s97", "s98", "s99", "scc", "memory")

constexpr int L_S = 0, L_W1 = 49152, L_END = 163840;
constexpr int L1_U = 0, L1_CT = 8192, L1_SO = 16384, L1_WI = 24576, L1_RT = 33792;
constexpr int L2_Z = L_S, L2_X = L_S + 8192, L2_E = L_S + 16384;
constexpr int L3_Y = L_S, L_U = L_S + 8192, L_RED = L_S;
constexpr int NWH2 = 28, NW2_A = 16, FLA = 11, NQ = 6, FLB = 1;

extern "C" __global__ __launch_bounds__(256, 1) __attribute__((aligned(4096)))
void fwd(const u16* __restrict__ x, const u16* __restrict__ wr, const u16* __restrict__ wi, const u16* __restrict__ w1,
         const u16* __restrict__ w2, char* __restrict__ ws, u16* __restrict__ out) {
  __shared__ __attribute__((aligned(16))) char lds[L_END];
  unsigned cpf;
  u32x4 xa[2];
  {
    unsigned s0, s1, v0, v1;
    asm volatile("s_getpc_b64 s[98:99]\n\t"
                 "s_lshr_b32 %1, %5, 3\n\ts_lshl_b32 %2, %1, 10\n\ts_min_u32 %2, %2, zz_end-fwd-1024\n\t"
                 "s_and_b32 s98, s98, 0xffffff00\n\ts_add_u32 s98, s98, %2\n\ts_addc_u32 s99, s99, 0\n\t"
                 "v_lshlrev_b32 %3, 2, %6\n\t"
                 "global_load_dword %0, %3, s[98:99]\n\t"
                 "s_and_b32 %1, %1, 7\n\ts_bfe_u32 %2, %5, 0x10000\n\ts_lshl3_add_u32 %1, %2, %1\n\t"
                 "s_lshr_b32 %2, %5, 6\n\ts_lshl_b32 %2, %2, 6\n\ts_add_u32 %1, %1, %2\n\t"
                 "s_and_b32 %2, %5, 6\n\ts_lshl_b32 %2, %2, 7\n\ts_add_u32 %1, %1, %2\n\ts_lshl_b32 %1, %1, 11\n\t"
                 "v_and_b32 %3, 63, %6\n\tv_lshl_add_u32 %3, %3, 4, %1\n\t"
                 "v_and_b32 %4, 64, %6\n\tv_lshl_add_u32 %3, %4, 10, %3\n\t"
                 "v_and_b32 %4, 0x80, %6\n\tv_lshl_add_u32 %3, %4, 3, %3\n\t"
                 "v_add_u32 %4, 0x8000, %3"
                 : "=&v"(cpf), "=&s"(s0), "=&s"(s1), "=&v"(v0), "=&v"(v1)
                 : "s"(blockIdx.x), "v"(threadIdx.x) : "s98", "s99", "scc", "memory");
    asm volatile("global_load_dwordx4 %0, %2, %4\n\tglobal_load_dwordx4 %1, %3, %4"
                 : "=&v"(xa[0]), "=&v"(xa[1]) : "v"(v0), "v"(v1), "s"(x) : "memory");
  }
  SCHED();
  const int tid = threadIdx.x, lane = tid & 63, w = __builtin_amdgcn_readfirstlane(tid >> 6);
  const int bid = blockIdx.x, xcd = bid & 7, bi = bid >> 3;
  const int j = lane & 15, g = lane >> 4;
  const int P = 4 * (xcd >> 1) + (bi >> 3), c = 8 * (xcd & 1) + (bi & 7), col = 32 * xcd + bi;
  const int rb = 2 * P + (xcd & 1), db = bi & 7;
  const rsrc_t rw = make_rsrc(ws, WS_USED);
  const u32x4 MK4 = {MARK, MARK, MARK, MARK};
  u32x2 xv[2][2];
  float xm[2];
  u64 tag;
  u32x4 r2v[4];
  const int u2 = w == 2 ? bid : min(256 + bid, 447);
  u32x4 F1R[4], F1I[4], F2R[2][2], F2I[2][2];
  f32x4 fc[2], fs[2], ic[2], is[2];
  {
    const int cg = bi & 3, rg = xcd + 8 * (bi >> 2), pw = 8 * (rg & 7) + 256 * (rg >> 3);
    u32x2 wa[2], wb_[2];
#pragma unroll
    for (int i = 0; i < 2; ++i) {
      const unsigned wo = (unsigned)((pw + w + 4 * i + 64 * g) * DF + 64 * cg + 4 * j);
      wa[i] = *(const u32x2u*)(wr + wo);
      wb_[i] = *(const u32x2u*)(wi + wo);
    }
    unsigned sp;
    if (w == 3) {
      const int pe = pw + 2 * cg + (lane >> 5) + 64 * ((lane >> 3) & 3), pp = (lane & 4) ? (S - pe) & (S - 1) : pe;
      asm volatile("global_load_ushort %0, %1, off"
                   : "=&v"(sp) : "v"(((lane & 1) ? wi : wr) + pp * DF + (lane & 2) * 128) : "memory");
    }
    SCHED();
#pragma unroll
    for (int odd = 0; odd < 2; ++odd) {
      const int na = w == 0 ? 16 : w == 3 ? 1 : 32, nb = w == 2 ? 240 : 0, o1 = odd ? (w == 3 ? -128 : 128) : 0, o2 = (odd && w == 1) ? 256 : 0;
      const unsigned ml = w == 2 ? 0xffffu : w == 3 ? 0u : 0xffffffffu, mh = w == 2 ? 0xffff0000u : ml, mf = w == 3 ? 0xffffffffu : 0u;
      const bool first = odd && (w & 1) == 0 && j == 0;
      const int n = first ? 256 : na * j - (j < 8 ? 0 : nb), olo = first ? 0 : o1 + (j < 8 ? 0 : o2);
      const int ohi = olo + (w == 0 ? n : w == 1 ? -128 : 0);
      const unsigned mask = j < 8 ? ml : mh;
      u32x4 o;
#pragma unroll
      for (int h = 0; h < 4; ++h) {
        const int mn = (4 * g + (w == 0 ? ((2 * h) & 3) + 16 * (h >> 1) : h)) * n;
        const float lo = cosr((float)(mn + olo) * (1.f / 512.f)), hi = cosr((float)(mn + ohi) * (1.f / 512.f));
        o[h] = (pk(lo, hi) & mask) | (__builtin_bit_cast(unsigned, lo) & mf);
      }
      *(u32x4*)(lds + L1_CT + (2 * w + odd) * 1024 + lane * 16) = o;
    }
    unsigned wdst[2];
#pragma unroll
    for (int i = 0; i < 2; ++i) wdst[i] = OFF_W + kslot(64 * cg + 32 * i + (tid >> 3)) * COLB + (8 * rg + (tid & 7)) * 16;
    BAR();
    const char* ct = lds + L1_CT + lane * 16;
    const u32x4 BR = *(const u32x4*)ct, BI = *(const u32x4*)(ct + 1024), AR = *(const u32x4*)(ct + 2048), AI = *(const u32x4*)(ct + 3072);
    const u32x4 SR = *(const u32x4*)(ct + 4096), SI = *(const u32x4*)(ct + 5120);
    const f32x4 tc = *(const f32x4*)(ct + 6144), ts = *(const f32x4*)(ct + 7168);
    SCHED();
    u32x4 rt[2];
    {
      const int ks = bid >> 4;
      const u16* s1 = w1 + (64 * (ks & 3) + 4 * (ks >> 2)) * F + 112 * (bid & 15);
#pragma unroll
      for (int i = 0; i < 2; ++i) {
        const unsigned e = min(w + 4 * i, 6) * 64 + lane, r = (e * 4682) >> 16, ch = e - 14 * r;
        rt[i] = *(const u32x4*)(s1 + ((16 * ((r >> 2) & 3) + (r & 3) + 256 * (r >> 4)) * F + 8 * ch));
      }
    }
    SCHED();
    PAGE(1);
    asm volatile("s_waitcnt vmcnt(6)" : "+v"(xa[0]), "+v"(xa[1]) :: "memory");
    char* wl = lds + L1_U + w * 2048;
    {
      const f32x4 v[2][2] = {{unlo(xa[0]), unhi(xa[0])}, {unlo(xa[1]), unhi(xa[1])}};
#pragma unroll
      for (int r = 0; r < 2; ++r) *(u32x4*)(wl + r * 1024 + lane * 16) = xa[r];
#pragma unroll
      for (int r = 0; r < 2; ++r)
#pragma unroll
        for (int at = 0; at < 2; ++at) xv[r][at] = *(const u32x2*)(wl + r * 1024 + at * 512 + j * 32 + g * 8);
      float mean, var;
      ln_sums2(v, lane, mean, var);
      xm[0] = dpp_get<0x00>(mean);
      xm[1] = dpp_get<0xAA>(mean);
      const float r_ = __builtin_amdgcn_rcpf(ln_sd(var)), n_ = -rbf(mean) * r_;
      const float rs[2] = {dpp_get<0x00>(r_), dpp_get<0xAA>(r_)}, nb[2] = {dpp_get<0x00>(n_), dpp_get<0xAA>(n_)};
#pragma unroll
      for (int r = 0; r < 2; ++r) *(u32x4*)(wl + r * 1024 + lane * 16) = pk4(v[r][0] * rs[r] + nb[r], v[r][1] * rs[r] + nb[r]);
    }
    SCHED();
    tag = __hip_atomic_load((gu64*)(ws + OFF_FL + 256 * 128), __ATOMIC_RELAXED, __HIP_MEMORY_SCOPE_AGENT) + 1;
#pragma unroll
    for (int i = 0; i < 2; ++i) st16(MK4, rw, tid * 16 + i * 4096, OFF_Y + bid * 8192);
    SCHED();
    u32x4 a[2];
#pragma unroll
    for (int r = 0; r < 2; ++r) {
      const u32x2 a0 = trread(wl + r * 1024 + lane * 8), a1 = trread(wl + r * 1024 + 512 + lane * 8);
      a[r] = (u32x4){a0[0], a0[1], a1[0], a1[1]};
    }
    unsigned* wim = (unsigned*)(lds + L1_WI);
#pragma unroll
    for (int i = 0; i < 2; ++i)
#pragma unroll
      for (int e = 0; e < 4; ++e) {
        const unsigned t = __builtin_amdgcn_perm(wb_[i][e >> 1], wa[i][e >> 1], (e & 1) ? 0x07060302u : 0x05040100u);
        wim[(4 * j + e) * 33 + 4 * (w + 4 * i) + g] = t;
      }
    f32x4 dr[2], di[2];
    PAD_IN2(a[0], a[1]);
#pragma unroll
    for (int r = 0; r < 2; ++r) { mma0(dr[r], a[r], BR); mma0(di[r], a[r], BI); }
    PAD_OUT4(dr[0], di[0], dr[1], di[1]);
    u32x4 t[2];
#pragma unroll
    for (int r = 0; r < 2; ++r) t[r] = pk4(dr[r] * tc + di[r] * ts, di[r] * tc - dr[r] * ts);
    f32x4 xr[2], xi[2], sr[2], si[2];
    PAD_IN2(t[0], t[1]);
#pragma unroll
    for (int r = 0; r < 2; ++r) { mma0(xr[r], AR, t[r]); mma0(xi[r], AI, t[r]); mma0(sr[r], SR, t[r]); mma0(si[r], SI, t[r]); }
    PAD_OUT4(xr[0], xi[0], sr[0], si[0]);
    asm volatile("" : "+v"(xr[1]), "+v"(xi[1]), "+v"(sr[1]), "+v"(si[1]));
    unsigned* so = (unsigned*)(lds + L1_SO);
#pragma unroll
    for (int r = 0; r < 2; ++r) {
      const u32x4 o1 = pk4(xr[r], xi[r]), o2 = pk4(sr[r], si[r]);
#pragma unroll
      for (int h = 0; h < 4; ++h) so[(2 * w + r) * 256 + 64 * h + lane] = j == 0 ? o2[h] : o1[h];
    }
#pragma unroll
    for (int i = 0; i < 2; ++i) *(u32x4*)(lds + L1_RT + (w + 4 * i) * 1024 + lane * 16) = rt[i];
    BAR();
#pragma unroll
    for (int i = 0; i < 2; ++i) {
      const unsigned* zi = so + (tid & 1) * 1024 + (tid >> 1) + 128 * i;
      st16((u32x4){zi[0], zi[256], zi[512], zi[768]}, rw, tid * 16 + i * 4096, OFF_Z + (P * 16 + c) * 8192);
    }
#pragma unroll
    for (int i = 0; i < 2; ++i) {
      const unsigned* wq = wim + (32 * i + (tid >> 3)) * 33 + (tid & 7) * 4;
      if (i > 0 || cg > 0 || tid >= 8) st16((u32x4){wq[0], wq[1], wq[2], wq[3]}, rw, wdst[i], 0);
    }
    if (w == 3) {
      asm volatile("" : "+v"(sp));
      const float v = bflo(sp), m = dpp_get<0x104>(v);
      const float t = (lane & 1) ? v - m : v + m, u = dpp_get<0x4E>(t);
      const float cv = 0.25f * ((lane & 2) ? u - t : u + t);
      const unsigned o = pk(cv, dpp_get<0xB1>(cv));
      if ((lane & 5) == 0) st4(o, rw, (lane >> 3) * 4 + (lane & 2) * 128 * COLB, OFF_W + (8 * rg + 2 * cg) * 16);
    }
    const char* r1 = lds + L1_RT + (4 * g + (j >> 2)) * 224 + (j & 3) * 8 + w * 32;
    const u32x2 a0 = trread(r1), a1 = trread(r1 + 3584), b0_ = trread(r1 + 128), b1 = trread(r1 + 3712);
    const unsigned o1 = OFF_W1 + ((bid & 15) * 16 + (bid >> 4)) * 7168 + w * 1024;
    st16((u32x4){a0[0], a0[1], a1[0], a1[1]}, rw, lane * 16, o1);
    st16((u32x4){b0_[0], b0_[1], b1[0], b1[1]}, rw, lane * 16, w < 3 ? o1 + 4096 : WS_USED);
    if (w >= 2) {
      const int k2 = u2 >> 3, q = (k2 * 37) >> 8, jj = k2 - 7 * q;
      const u16* s0 = w2 + (224 * q + (int)((0x60b09070402000ull >> (8 * jj)) & 255)) * D + 64 * (u2 & 7);
      const u16* s1 = s0 + (jj == 6 ? 112 : 16) * D;
      asm volatile("global_load_dwordx4 %0, %4, %5\n\tglobal_load_dwordx4 %1, %4, %6\n\tglobal_load_dwordx4 %2, %4, %7\n\t"
                   "global_load_dwordx4 %3, %4, %8\n\ts_waitcnt vmcnt(4)"
                   : "=&v"(r2v[0]), "=&v"(r2v[1]), "=&v"(r2v[2]), "=&v"(r2v[3])
                   : "v"((lane >> 3) * 1024 + (lane & 7) * 16), "s"(s0), "s"(s0 + 8 * D), "s"(s1), "s"(s1 + 8 * D) : "memory");
    } else {
      asm volatile("s_waitcnt vmcnt(0)" ::: "memory");
    }
    asm volatile("" :: "v"(cpf));
    BAR();
    if (tid == 0) __hip_atomic_store((gu64*)(ws + OFF_FL + bid * 128), tag, __ATOMIC_RELAXED, __HIP_MEMORY_SCOPE_AGENT);
    if (w >= 2) {
#pragma unroll
      for (int i = 0; i < 4; ++i) st16(MK4, rw, lane * 16 + i * 1024, OFF_A1 + bid * 8192 + (w - 2) * 4096);
      st16(MK4, rw, lane < 2 ? lane * 16 : WS_USED, OFF_RS + bid * 64 + (w - 2) * 32);
    }
  }
  const unsigned w1l = (unsigned)(size_t)(LDSP char*)lds + L_W1 + w * 28672;
  const char* w1g = ws + OFF_W1 + c * 114688 + w * 28672;
  u32x4 wb[14][4];
  const unsigned w2off = OFF_W2 + (db * 56 + 14 * w) * 4096;
#define W1_DMA(i) do { if ((i) % 8 == 0) asm volatile("s_mov_b32 m0, %0\n\ts_nop 0" :: "s"(w1l + ((i) + 4) * 1024) : "memory");                        \
    asm volatile("global_load_lds_dwordx4 %0, %1 offset:%2 sc1" :: "v"(lane * 16), "s"(w1g + ((i) / 8 * 8 + 4) * 1024), "n"(((i) % 8 - 4) * 1024) : "memory"); } while (0)
#define WL(n) do { SCHED(); if ((n) < 28) W1_DMA(n); else W2_LD((n) - 28); SCHED(); } while (0)
#define W2_LD(n) (wb[(n) >> 2][(n) & 3] = ld16(rw, lane * 16 + ((n) & 3) * 1024, w2off + ((n) >> 2) * 4096))
  {
#pragma unroll
    for (int h = 0; h < 4; ++h)
#pragma unroll
      for (int bt = 0; bt < 2; ++bt) {
        const float a2 = (float)((16 * bt + j) * (16 * w + 4 * g + h)) * (1.f / 2048.f);
        ic[bt][h] = cosr(a2);
        is[bt][h] = sinr(a2);
      }
    asm volatile("" : "+v"(ic[0]), "+v"(ic[1]), "+v"(is[0]), "+v"(is[1]));
    {
      const int k1 = 16 * w + j;
      const unsigned m1 = (j & 1) ? SS : 0u, mr = (j & 2) ? SS : 0u, mi = ((j + 1) & 2) ? SS : 0u;
#pragma unroll
      for (int h = 0; h < 4; ++h) {
        const float ang = (float)((4 * g + h) * k1) * (1.f / 64.f);
        const float cs = cosr(ang), sn = sinr(ang);
        const unsigned q0 = pk(cs, sn), i0 = pk(-sn, cs);
        F1R[0][h] = q0; F1I[0][h] = i0;
        F1R[2][h] = q0 ^ m1; F1I[2][h] = i0 ^ m1;
        F1R[1][h] = ((j & 1) ? i0 : q0) ^ mr; F1I[1][h] = ((j & 1) ? q0 : i0) ^ mi;
        F1R[3][h] = F1R[1][h] ^ m1; F1I[3][h] = F1I[1][h] ^ m1;
        const float an2 = (float)((4 * g + h) * j) * (1.f / 32.f);
        const float c2 = cosr(an2), s2 = sinr(an2);
        const unsigned r2 = pk(c2, s2), i2 = pk(-s2, c2), mh = (h & 1) ? SS : 0u;
        F2R[0][0][h] = r2; F2I[0][0][h] = i2;
        F2R[0][1][h] = r2 ^ m1; F2I[0][1][h] = i2 ^ m1;
        F2R[1][0][h] = r2 ^ mh; F2I[1][0][h] = i2 ^ mh;
        F2R[1][1][h] = r2 ^ m1 ^ mh; F2I[1][1][h] = i2 ^ m1 ^ mh;
#pragma unroll
        for (int bt = 0; bt < 2; ++bt) {
          const float a1 = (float)((16 * bt + 4 * g + h) * k1) * (1.f / 2048.f);
          fc[bt][h] = cosr(a1);
          fs[bt][h] = sinr(a1);
        }
      }
    }
#pragma unroll
    for (int t = 0; t < 4; ++t) asm volatile("" : "+v"(F1R[t]), "+v"(F1I[t]), "+v"(F2R[t >> 1][t & 1]), "+v"(F2I[t >> 1][t & 1]));
    asm volatile("" : "+v"(fc[0]), "+v"(fc[1]), "+v"(fs[0]), "+v"(fs[1]));
    SCHED();
    WARM(2); WARM(3); WARM(4); WARM(5); WARM(6);
    if (w >= 2) {
      asm volatile("s_waitcnt vmcnt(5)" : "+v"(r2v[0]), "+v"(r2v[1]), "+v"(r2v[2]), "+v"(r2v[3]) :: "memory");
      char* im = lds + L1_RT + 8192 + (w - 2) * 4096;
#pragma unroll
      for (int i = 0; i < 4; ++i) *(u32x4*)(im + i * 1024 + lane * 16) = r2v[i];
      const char* r2 = im + (4 * g + (j >> 2)) * 128 + (j & 3) * 32;
      const unsigned o2 = (w == 3 && bid >= 192) ? WS_USED : OFF_W2 + ((u2 & 7) * 56 + (u2 >> 3)) * 4096;
#pragma unroll
      for (int dt = 0; dt < 4; ++dt) {
        const u32x2 lo = trread(r2 + dt * 8), hi = trread(r2 + 2048 + dt * 8);
        st16((u32x4){lo[0], lo[1], hi[0], hi[1]}, rw, lane * 16 + dt * 1024, o2);
      }
    } else {
      for (int sp = 0; sp < SPIN_MAX; ++sp) {
        const u64 f0 = __hip_atomic_load((gu64*)(ws + OFF_FL + tid * 128), __ATOMIC_RELAXED, __HIP_MEMORY_SCOPE_AGENT);
        const u64 f1 = __hip_atomic_load((gu64*)(ws + OFF_FL + 16384 + tid * 128), __ATOMIC_RELAXED, __HIP_MEMORY_SCOPE_AGENT);
        if (__all(f0 == tag && f1 == tag)) break;
      }
    }
    BAR();
    if (bid == 0 && tid == 0) __hip_atomic_store((gu64*)(ws + OFF_FL + 256 * 128), tag, __ATOMIC_RELAXED, __HIP_MEMORY_SCOPE_AGENT);
    u32x4 zv[2], wv[2], wv2[2];
#pragma unroll
    for (int i = 0; i < 2; ++i) zv[i] = ld16(rw, (tid >> 1) * 8192 + (tid & 1) * 16, OFF_Z + i * 128 * 8192 + col * 32);
#pragma unroll
    for (int kt = 0; kt < 2; ++kt) wv[kt] = ld16(rw, (g * 64 + j) * 16, OFF_W + col * COLB + kt * 4096 + w * 256);
#pragma unroll
    for (int kt = 0; kt < 2; ++kt) wv2[kt] = ld16(rw, (g * 64 + j) * 16, OFF_W + (col == 0 ? 256 : col) * COLB + kt * 4096 + w * 256);
    SCHED();
    PAGE(2);
#pragma unroll
    for (int i = 0; i < 2; ++i) *(u32x4*)(lds + L2_Z + i * 4096 + tid * 16) = zv[i];
    asm volatile("s_waitcnt vmcnt(0)" : "+v"(wv[0]), "+v"(wv[1]), "+v"(wv2[0]), "+v"(wv2[1]) :: "memory");
    f32x4 a2[2], b2[2];
    unsigned exw[2], exr[2];
    if (col == 0) {
      const bool k0 = tid == lane && j == 0;
#pragma unroll
      for (int kt = 0; kt < 2; ++kt) {
        a2[kt] = unlo(wv2[kt]);
        b2[kt] = unhi(wv2[kt]);
        exw[kt] = L2_E + (k0 ? 8204 + g * 16 + kt * 64 : (g * 64 + 16 * w + j) * 16 + kt * 4096);
        exr[kt] = L2_E + (k0 ? 8192 + 128 - g * 16 - kt * 64 : ((3 - g) * 64 + 64 - 16 * w - j) * 16 + 4096 - kt * 4096);
      }
      asm volatile("" : "+v"(a2[0]), "+v"(a2[1]), "+v"(b2[0]), "+v"(b2[1]), "+v"(exw[0]), "+v"(exw[1]), "+v"(exr[0]), "+v"(exr[1]));
    }
    BAR();
    u32x4 za[2][4];
#pragma unroll
    for (int n = 0; n < 8; ++n) za[n >> 2][n & 3] = *(const u32x4*)(lds + L2_Z + (g * 32 + j) * 16 + (n & 3) * 2048 + (n >> 2) * 256);
    SCHED();
    f32x4 re[2], im[2], fa[2], fb[2];
#pragma unroll
    for (int bt = 0; bt < 2; ++bt)
#pragma unroll
      for (int t = 0; t < 4; ++t) {
        if (t == 0) { mma0(re[bt], za[bt][t], F1R[t]); mma0(im[bt], za[bt][t], F1I[t]); }
        else { mma(re[bt], za[bt][t], F1R[t]); mma(im[bt], za[bt][t], F1I[t]); }
        if (bt == 1) {
          if (t & 1) fb[t >> 1] = unhi(wv[t >> 1]); else fa[t >> 1] = unlo(wv[t >> 1]);
          SCHED();
        } else {
          WL(t);
        }
      }
    asm volatile("" : "+v"(fa[0]), "+v"(fa[1]), "+v"(fb[0]), "+v"(fb[1]), "+v"(re[0]), "+v"(im[0]));
    u32x4 tf[2];
    f32x4 xr[2], xi[2];
    tf[0] = pk4(re[0] * fc[0] + im[0] * fs[0], im[0] * fc[0] - re[0] * fs[0]);
    asm volatile("s_nop 1" : "+v"(tf[0]), "+v"(re[1]), "+v"(im[1]));
#pragma unroll
    for (int kt = 0; kt < 2; ++kt) { mma0(xr[kt], F2R[kt][0], tf[0]); mma0(xi[kt], F2I[kt][0], tf[0]); }
    WL(4);
    tf[1] = pk4(re[1] * fc[1] + im[1] * fs[1], im[1] * fc[1] - re[1] * fs[1]);
    asm volatile("s_nop 1" : "+v"(tf[1]));
#pragma unroll
    for (int kt = 0; kt < 2; ++kt) { mma(xr[kt], F2R[kt][1], tf[1]); mma(xi[kt], F2I[kt][1], tf[1]); }
    WL(5); WL(6);
    asm volatile("s_nop 3" : "+v"(xr[0]), "+v"(xi[0]));
    u32x4 yf[2];
    if (col == 0) {
      asm volatile("s_nop 1" : "+v"(xr[1]), "+v"(xi[1]));
#pragma unroll
      for (int kt = 0; kt < 2; ++kt) {
        const u32x4 z = pk4(xr[kt], xi[kt]);
        *(u32x4*)(lds + exw[kt]) = z;
        if (kt == 0 && tid == 0) *(unsigned*)(lds + L2_E + 8204 + 128) = z[0];
      }
      BAR();
      const u32x4 q[2] = {*(const u32x4*)(lds + exr[0]), *(const u32x4*)(lds + exr[1])};
      SCHED();
      f32x4 ur[2], ui[2];
#pragma unroll
      for (int kt = 0; kt < 2; ++kt) { ur[kt] = xr[kt] * fa[kt] - xi[kt] * fb[kt]; ui[kt] = xr[kt] * fb[kt] + xi[kt] * fa[kt]; }
      SCHED();
#pragma unroll
      for (int kt = 0; kt < 2; ++kt) {
        const u32x4 m = {q[kt][3], q[kt][2], q[kt][1], q[kt][0]};
        const f32x4 mr = unlo(m), mi = unhi(m);
        yf[kt] = pk4(mr * b2[kt] + (ui[kt] - mi * a2[kt]), mi * b2[kt] + (mr * a2[kt] + ur[kt]));
      }
    } else {
      yf[0] = pk4(xr[0] * fb[0] + xi[0] * fa[0], xr[0] * fa[0] - xi[0] * fb[0]);
    }
    asm volatile("s_nop 1" : "+v"(yf[0]), "+v"(xr[1]), "+v"(xi[1]));
#pragma unroll
    for (int bt = 0; bt < 2; ++bt) { mma0(im[bt], yf[0], F2R[bt][0]); mma0(re[bt], yf[0], F2I[bt][0]); }
    WL(7);
    if (col != 0) yf[1] = pk4(xr[1] * fb[1] + xi[1] * fa[1], xr[1] * fa[1] - xi[1] * fb[1]);
    asm volatile("s_nop 1" : "+v"(yf[1]));
#pragma unroll
    for (int bt = 0; bt < 2; ++bt) { mma(im[bt], yf[1], F2R[bt][1]); mma(re[bt], yf[1], F2I[bt][1]); }
    WL(8); WL(9);
    asm volatile("s_nop 3" : "+v"(re[0]), "+v"(im[0]));
    *(u32x4*)(lds + L2_X + (w * 128 + lane) * 16) = pk4(im[0] * ic[0] + re[0] * is[0], re[0] * ic[0] - im[0] * is[0]);
    WL(10);
    asm volatile("" : "+v"(re[1]), "+v"(im[1]));
    *(u32x4*)(lds + L2_X + (w * 128 + 64 + lane) * 16) = pk4(im[1] * ic[1] + re[1] * is[1], re[1] * ic[1] - im[1] * is[1]);
    WL(11);
    BAR();
    u32x4 u[2][4];
#pragma unroll
    for (int n = 0; n < 8; ++n) u[n >> 2][n & 3] = *(const u32x4*)(lds + L2_X + (((n & 3) * 2 + (n >> 2)) * 64 + lane) * 16);
    SCHED();
#pragma unroll
    for (int bt = 0; bt < 2; ++bt)
#pragma unroll
      for (int t = 0; t < 4; ++t) {
        if (t == 0) { mma0(im[bt], F1R[t], u[bt][t]); mma0(re[bt], F1I[t], u[bt][t]); }
        else { mma(im[bt], F1R[t], u[bt][t]); mma(re[bt], F1I[t], u[bt][t]); }
        if (bt == 0) WL(12 + t);
      }
    asm volatile("" : "+v"(re[0]), "+v"(im[0]));
    st16(pk4(re[0], im[0]), rw, (g * 32 + j) * 16, OFF_Y + col * COLB + w * 2048);
    asm volatile("s_nop 3" : "+v"(re[1]), "+v"(im[1]));
    st16(pk4(re[1], im[1]), rw, (g * 32 + j) * 16 + 256, OFF_Y + col * COLB + w * 2048);
  }
  PAGE(3);
  f32x4 h[2][2];
  float mbs;
  unsigned rdst;
  {
#pragma unroll
    for (int i = 16; i < NWH2; ++i) WL(i);
    u32x4 BR, BI, SR, SI, C2[2];
    f32x4 tc, ts;
    {
      constexpr float SC = 1.f / 1048576.f;
      const unsigned up = g < 2 ? 0x80000000u : 0u, lo8 = g < 2 ? 0xffffffffu : 0u;
#pragma unroll
      for (int h = 0; h < 4; ++h) {
        const float kj = (float)((4 * g + h) * j);
        const float c1 = cosr(kj * (1.f / 16.f)) * SC, s1 = sinr(kj * (1.f / 16.f)) * SC;
        BR[h] = pk(c1, s1) ^ up;
        BI[h] = pk(s1, c1) ^ up ^ 0x80000000u;
        const float ph = kj * (1.f / 16.f) - (g < 2 ? 0.f : (float)j * (15.f / 32.f));
        unsigned sv = pk(cosr(ph) * (2.f * SC), sinr(ph) * (-2.f * SC));
        unsigned cv = pk(cosr(kj * (1.f / 32.f)) * 2.f, sinr(kj * (1.f / 32.f)) * -2.f);
        if (h == 0) { sv = g == 0 ? pk(SC, (j & 1) ? -SC : SC) : sv; cv = g == 0 ? pk(1.f, (j & 1) ? -1.f : 1.f) : cv; }
        SR[h] = sv & lo8;
        SI[h] = sv & ~lo8;
        C2[0][h] = cv;
        C2[1][h] = cv ^ ((h & 1) ? SS : 0u);
        tc[h] = cosr(kj * (1.f / 512.f));
        ts[h] = sinr(kj * (1.f / 512.f));
      }
      asm volatile("" : "+v"(BR), "+v"(BI), "+v"(SR), "+v"(SI), "+v"(C2[0]), "+v"(C2[1]), "+v"(tc), "+v"(ts));
    }
#pragma unroll
    for (int r = 0; r < 2; ++r)
#pragma unroll
      for (int at = 0; at < 2; ++at)
        h[r][at] = (f32x4){bflo(xv[r][at][0]), bfhi(xv[r][at][0]), bflo(xv[r][at][1]), bfhi(xv[r][at][1])};
    unsigned lyw = L3_Y + (tid & 1) * 4096 + (tid >> 1) * 4, lyr = L3_Y + w * 2048 + lane * 4, ly0 = L3_Y + w * 2048;
    unsigned luw[2] = {(unsigned)(L_U + lane * 128 + ((2 * w) ^ (lane & 7)) * 16), (unsigned)(L_U + lane * 128 + ((2 * w + 1) ^ (lane & 7)) * 16)};
    unsigned lur = L_U + (tid >> 3) * 128 + ((tid ^ (tid >> 3)) & 7) * 16;
    unsigned adst = OFF_A1 + (8 * P + (c >> 1)) * 16384 + (c & 1) * 128 + (tid >> 5) * 1024 + ((tid >> 3) & 3) * 256 + (tid & 7) * 16;
    rdst = (lane & 61) == 0 ? OFF_RS + (128 * P + 8 * c + 2 * w) * 8 + lane * 4 : WS_USED;
    asm volatile("" : "+v"(h[0][0]), "+v"(h[0][1]), "+v"(h[1][0]), "+v"(h[1][1]), "+v"(lyw), "+v"(lyr), "+v"(ly0), "+v"(luw[0]), "+v"(luw[1]),
                      "+v"(lur), "+v"(adst), "+v"(rdst));
    SCHED();
    u32x4 yv[2];
    for (int sp = 0; sp < SPIN_MAX; ++sp) {
      FENCE();
#pragma unroll
      for (int i = 0; i < 2; ++i) yv[i] = ld16(rw, (tid >> 1) * COLB + (tid & 1) * 16, OFF_Y + i * 128 * COLB + (P * 32 + 2 * c) * 16);
      if (!__any(mark4(mark4(0u, yv[0]), yv[1]) == MARK)) break;
    }
#pragma unroll
    for (int i = 0; i < 2; ++i)
#pragma unroll
      for (int e = 0; e < 4; ++e) *(unsigned*)(lds + lyw + e * 1024 + i * 512) = yv[i][e];
    BAR();
    u32x4 a[2];
    unsigned y0[2];
#pragma unroll
    for (int r = 0; r < 2; ++r) {
      const unsigned* yp = (const unsigned*)(lds + lyr + r * 1024);
      a[r] = (u32x4){yp[0], yp[64], yp[128], yp[192]};
    }
#pragma unroll
    for (int r = 0; r < 2; ++r) y0[r] = *(const unsigned*)(lds + ly0 + r * 1024);
    WL(NWH2);
    f32x4 ur[2], ui[2], sr[2], si[2];
#pragma unroll
    for (int r = 0; r < 2; ++r) { mma0(ur[r], a[r], BR); mma0(ui[r], a[r], BI); mma0(sr[r], a[r], SR); mma0(si[r], a[r], SI); WL(NWH2 + 1 + r); }
    const float mb0 = rbf(bflo(y0[0]) * (1.f / 1048576.f) + xm[0]), mb1 = rbf(bflo(y0[1]) * (1.f / 1048576.f) + xm[1]);
    h[0][0] -= mb0; h[0][1] -= mb0; h[1][0] -= mb1; h[1][1] -= mb1;
    asm volatile("" : "+v"(h[0][0]), "+v"(h[0][1]), "+v"(h[1][0]), "+v"(h[1][1]), "+v"(ur[0]), "+v"(ui[0]), "+v"(sr[0]), "+v"(si[0]),
                      "+v"(ur[1]), "+v"(ui[1]), "+v"(sr[1]), "+v"(si[1]));
#pragma unroll
    for (int r = 0; r < 2; ++r) {
      if (g == 0) { ur[r][0] = sr[r][0]; ui[r][0] = si[r][0]; }
      u32x4 t = pk4(ur[r] * tc - ui[r] * ts, ui[r] * tc + ur[r] * ts);
      asm volatile("s_nop 1" : "+v"(t));
      mma(h[r][0], t, C2[0]); mma(h[r][1], t, C2[1]);
      WL(NWH2 + 3 + r);
    }
#pragma unroll
    for (int r = 0; r < 2; ++r) {
      asm volatile("s_nop %2" : "+v"(h[r][0]), "+v"(h[r][1]) : "n"(r ? 1 : 0));
      *(u32x4*)(lds + luw[r]) = (u32x4){pk(h[r][0][0], h[r][0][1]), pk(h[r][0][2], h[r][0][3]), pk(h[r][1][0], h[r][1][1]), pk(h[r][1][2], h[r][1][3])};
      SCHED();
    }
    BAR();
    {
      const u32x4 u0 = *(const u32x4*)(lds + lur), u1 = *(const u32x4*)(lds + lur + 4096);
      st16(u0, rw, adst, 0);
      st16(u1, rw, adst + 8192, 0);
    }
    mbs = (lane & 2) ? mb1 : mb0;
    WARM(5); WARM(6);
  }
#pragma unroll
  for (int n = NWH2 + 5; n < 28 + NW2_A; ++n) WL(n);
  SCHED();
  PAGE(4);
  u32x4 hf[14][4];
  unsigned hoff[4];
#pragma unroll
  for (int m = 0; m < 4; ++m) hoff[m] = OFF_H + (4 * rb + ((w + m) & 3)) * 57344 + 14 * w * 1024;
  constexpr int KSO[14] = {0, 1, 3, 4, 7, 8, 10, 11, 2, 5, 6, 9, 12, 13};
#define H_LD(n) (hf[(n) >> 2][(n) & 3] = ld16(rw, lane * 16 + (KSO[(n) >> 2] & 3) * 1024, hoff[(n) & 3] + (KSO[(n) >> 2] >> 2) * 4096))
  const unsigned fget = (lane & 3) * 512 + ((lane >> 2) & 1) * 128, fgs = OFF_FH + ((P * 16 + 4 * w) * 4 + 2 * (rb & 1)) * 128;
  const unsigned fput = OFF_FH + ((P * 16 + c) * 4 + w) * 128;
  const u32x2 tag2 = __builtin_bit_cast(u32x2, tag);
  u32x2 fa;
  unsigned mv;
  {
    const unsigned asoff = OFF_A1 + (8 * P + 2 * w) * 16384;
    const unsigned hsoff = OFF_H + (8 * P + 2 * w) * 57344 + 7 * (c >> 1) * 1024;
#define A_LD(n) (a[(n) >> 1][(n) & 1] = ld16(rw, lane * 16 + (((n) >> 1) & 3) * 1024, asoff + ((n) & 1) * 16384 + ((n) >> 3) * 4096))
#define B_RD(ks, t) (*(const u32x4*)(((ks) < 8 ? wl0 : wl1) + (((ks) & 7) * 7 + (t)) * 1024))
    u32x4 a[16][2];
    f32x4 acc[2][4];
    unsigned lo_off = L_W1 + lane * 16, hi_off = L_W1 + 57344 + lane * 16;
    asm volatile("" : "+v"(lo_off), "+v"(hi_off));
    const char* wl0 = lds + lo_off;
    const char* wl1 = lds + hi_off;
    int sp = 0;
#define A_RELOAD() RELOAD16(a[0][0], a[0][1], a[4][0], a[4][1], a[1][0], a[1][1], a[5][0], a[5][1], a[2][0], a[2][1], a[6][0], a[6][1], \
                            a[3][0], a[3][1], a[7][0], a[7][1], asoff, asoff + 16384, asoff + 4096, asoff + 20480)
#pragma unroll
    for (int n = 0; n < 16; ++n) A_LD(n);
    FENCE();
    SCHED();
    u32x4 bf[3][4];
#pragma unroll
    for (int t = 0; t < 8; ++t) bf[t >> 2][t & 3] = B_RD(t >> 2, t & 3);
    SCHED();
    {
      float cm, var;
      ln_sums2(h, lane, cm, var);
      st8((u32x2){__builtin_bit_cast(unsigned, mbs), __builtin_bit_cast(unsigned, ln_sd(var))}, rw, rdst, 0);
    }
    SCHED();
    {
      unsigned mk = 0;
#pragma unroll
      for (int n = 0; n < 8; ++n) mk = mark4(mk, a[n >> 1][n & 1]);
      if (__builtin_expect(__any(mk == MARK), 0)) {
        do {
          A_RELOAD();
          mk = 0;
#pragma unroll
          for (int n = 0; n < 8; ++n) mk = mark4(mk, a[n >> 1][n & 1]);
        } while (__any(mk == MARK && ++sp < SPIN_MAX));
      }
    }
    unsigned rsv[2];
    const unsigned rsoff = OFF_RS + (8 * P + 2 * w) * 128;
    for (;;) {
      unsigned mk = 0;
#pragma unroll
      for (int ks = 0; ks < 16; ++ks) {
        if (ks > 0) asm volatile("" : "+v"(bf[ks % 3][0]), "+v"(bf[ks % 3][1]), "+v"(bf[ks % 3][2]), "+v"(bf[ks % 3][3]), "+v"(a[ks][0]), "+v"(a[ks][1]));
#pragma unroll
        for (int t = 0; t < 4; ++t) {
          if (ks < 8 && (t & 1)) A_LD(16 + 2 * ks + (t >> 1));
          if (ks >= 8 && (t & 1)) W2_LD(NW2_A + 2 * (ks - 8) + (t >> 1));
          if (ks == 11 && t == 0) { rsv[0] = ld4(rw, j * 8 + 4, rsoff); rsv[1] = ld4(rw, j * 8 + 132, rsoff); mv = ld4(rw, j * 8, OFF_RS + (4 * rb + w) * 128); }
          if (ks < 14) bf[(ks + 2) % 3][t] = B_RD(ks + 2, t);
          if (ks >= 4 && t < 2) mk = mark4(mk, a[ks][t]);
#pragma unroll
          for (int m = 0; m < 2; ++m) {
            if (ks == 0) mma0(acc[m][t], bf[0][t], a[0][m]);
            else mma(acc[m][t], bf[ks % 3][t], a[ks][m]);
          }
          SCHED();
        }
      }
      asm volatile("s_nop 7");
      if (__builtin_expect(!__any(mk == MARK) || ++sp >= SPIN_MAX, 1)) break;
      sp += 32;
      A_RELOAD();
#pragma unroll
      for (int t = 0; t < 8; ++t) bf[t >> 2][t & 3] = B_RD(t >> 2, t & 3);
    }
    if (__builtin_expect(__any(umax3(rsv[0], rsv[1], mv) == MARK), 0)) {
      unsigned n_;
      asm volatile("s_mov_b32 %3, 0x20000\n"
                   "1:\n\t"
                   "buffer_load_dword %0, %5, %4, %6 offen offset:4 sc1\n\tbuffer_load_dword %1, %5, %4, %6 offen offset:132 sc1\n\t"
                   "buffer_load_dword %2, %5, %4, %7 offen sc1\n\t"
                   "s_waitcnt vmcnt(0)\n\t"
                   "v_cmp_eq_u32 vcc, -1, %0\n\tv_cmp_eq_u32 s[96:97], -1, %1\n\ts_or_b64 s[96:97], s[96:97], vcc\n\t"
                   "v_cmp_eq_u32 vcc, -1, %2\n\ts_or_b64 vcc, s[96:97], vcc\n\t"
                   "s_sub_u32 %3, %3, 1\n\t"
                   "s_cbranch_scc1 2f\n\t"
                   "s_cbranch_vccnz 1b\n"
                   "2:"
                   : "=&v"(rsv[0]), "=&v"(rsv[1]), "=&v"(mv), "=&s"(n_)
                   : "s"(rw), "v"(j * 8), "s"(rsoff), "s"(OFF_RS + (4 * rb + w) * 128) : "vcc", "scc", "s96", "s97", "memory");
    }
    PAGE(5);
    const float sd[2] = {__builtin_bit_cast(float, rsv[0]), __builtin_bit_cast(float, rsv[1])};
    const float sc[2] = {-1.44269504088896f * __builtin_amdgcn_rcpf(sd[0]), -1.44269504088896f * __builtin_amdgcn_rcpf(sd[1])};
#pragma unroll
    for (int m = 0; m < 2; ++m) {
      u32x2 p[2];
#pragma unroll
      for (int t = 0; t < 4; ++t) {
        const f32x4 s = silu4(acc[m][t], sd[m], sc[m]);
        p[t & 1] = (u32x2){pk(s[0], s[1]), pk(s[2], s[3])};
        if (t & 1) st16((u32x4){p[0][0], p[0][1], p[1][0], p[1][1]}, rw, lane * 16 + (t >> 1) * 1024, hsoff + m * 57344 + (c & 1) * 3072);
        W2_LD(NW2_A + 16 + 4 * m + t);
        if (t > 0) bf[m][t - 1] = B_RD(m, 3 + t);
        SCHED();
      }
    }
#pragma unroll
    for (int ks = 0; ks < 16; ++ks) {
      if (ks == FLA) {
        asm volatile("s_waitcnt vmcnt(%0)" :: "n"(FLA) : "memory");
        if (lane == 0) st8(tag2, rw, 0, fput);
        SCHED();
      }
      asm volatile("" : "+v"(bf[ks % 3][0]), "+v"(bf[ks % 3][1]), "+v"(bf[ks % 3][2]));
#pragma unroll
      for (int t = 0; t < 3; ++t) {
        if (t == 0) W2_LD(NW2_A + 24 + ks);
        if (ks < 14) bf[(ks + 2) % 3][t] = B_RD(ks + 2, 4 + t);
#pragma unroll
        for (int m = 0; m < 2; ++m) {
          if (ks == 0) mma0(acc[m][t], bf[0][t], a[0][m]);
          else mma(acc[m][t], bf[ks % 3][t], a[ks][m]);
        }
        SCHED();
      }
    }
    asm volatile("s_nop 7" : "+v"(acc[0][0]), "+v"(acc[0][1]), "+v"(acc[0][2]), "+v"(acc[1][0]), "+v"(acc[1][1]), "+v"(acc[1][2]));
#pragma unroll
    for (int m = 0; m < 2; ++m) {
      u32x2 p[3];
      if (m == 1) fa = ld8(rw, fget, fgs);
#pragma unroll
      for (int t = 0; t < 3; ++t) {
        const f32x4 s = silu4(acc[m][t], sd[m], sc[m]);
        p[t] = (u32x2){pk(s[0], s[1]), pk(s[2], s[3])};
        if (t == 1) st16((u32x4){p[0][0], p[0][1], p[1][0], p[1][1]}, rw, lane * 16 + 2048, hsoff + m * 57344 + (c & 1) * 3072);
        if (t == 2) st8(p[2], rw, lane * 16, hsoff + m * 57344 + 6144 + (c & 1) * 8);
        SCHED();
      }
    }
    if (__builtin_expect(__any(__builtin_bit_cast(u64, fa) != tag), 0)) SPIN_FLAG(fget, fgs);
    FENCE();
#pragma unroll
    for (int n = 0; n < NQ; ++n) H_LD(n);
    asm volatile("s_waitcnt vmcnt(%0)" :: "n"(NQ) : "memory");
    if (lane == 0) st8(tag2, rw, 64, fput);
    FENCE();
#pragma unroll
    for (int n = NQ; n < 16; ++n) H_LD(n);
    FENCE();
    SCHED();
  }
  PAGE(6);
  {
#define RED_WR(m, t) (*(f32x4*)(lds + L_RED + (((((w + (m)) & 3) * 3 + (m) - 1) * 4 + (t)) * 64 + lane) * 16) = acc[m][t])
    const int rt = 4 * rb + w;
    u32x2 hr[4];
    f32x4 acc[4][4];
    asm volatile("s_barrier" ::: "memory");
    u32x2 fb;
#pragma unroll
    for (int ks = 0; ks < 14; ++ks) {
      if (ks == FLB) fb = ld8(rw, fget + 64, fgs);
      if (ks == 4) {
        if (__builtin_expect(__any(__builtin_bit_cast(u64, fb) != tag), 0)) SPIN_FLAG(fget + 64, fgs);
        FENCE();
      }
#pragma unroll
      for (int mm = 0; mm < 4; ++mm) {
        const int m = ks == 13 ? (mm + 1) & 3 : mm;
        if (ks < 10) H_LD(16 + 4 * ks + m);
        else if (ks == 10) hr[m] = ld8(rw, g * 256 + j * 16 + (db >> 2) * 8, OFF_A1 + rt * 16384 + (4 * m + (db & 3)) * 1024);
#pragma unroll
        for (int t = 0; t < 4; ++t) {
          if (ks == 0) mma0a(acc[m][t], wb[0][t], hf[0][m]);
          else mmaa(acc[m][t], wb[KSO[ks]][t], hf[ks][m]);
          if (ks == 13 && mm >= 2) { SCHED(); RED_WR(mm - 1, t); SCHED(); }
        }
        SCHED();
      }
    }
#pragma unroll
    for (int t = 0; t < 4; ++t) RED_WR(3, t);
    SCHED();
    asm volatile("s_nop 7" : "+v"(acc[0][0]), "+v"(acc[0][1]), "+v"(acc[0][2]), "+v"(acc[0][3]));
    f32x4 ho[4];
#pragma unroll
    for (int t = 0; t < 4; ++t) ho[t] = (f32x4){bflo(hr[t][0]), bfhi(hr[t][0]), bflo(hr[t][1]), bfhi(hr[t][1])} + (acc[0][t] + __builtin_bit_cast(float, mv));
    BAR();
    unsigned o[8];
#pragma unroll
    for (int t = 0; t < 4; ++t) {
      f32x4 s = ho[t];
#pragma unroll
      for (int v = 0; v < 3; ++v) s += *(const f32x4*)(lds + L_RED + (((w * 3 + v) * 4 + t) * 64 + lane) * 16);
      o[2 * t] = pk(s[0], s[1]);
      o[2 * t + 1] = pk(s[2], s[3]);
    }
    const int srow = 128 * (rt >> 3) + 32 * (j & 3) + 4 * (rt & 7) + (j >> 2);
    u16* op = out + (unsigned)(srow * D + 64 * db + 16 * g);
    *(u32x4*)op = (u32x4){o[0], o[1], o[2], o[3]};
    *(u32x4*)(op + 8) = (u32x4){o[4], o[5], o[6], o[7]};
  }
}

extern "C" __global__ void zz_end(char* ws) { if (ws == nullptr) ws[0] = 1; }

extern "C" void kernel_launch(void* const* d_in, const int* in_sizes, int n_in, void* d_out, int out_size, void* d_ws,
                              size_t ws_size, hipStream_t stream) {
  fwd<<<256, 256, 0, stream>>>((const u16*)d_in[0], (const u16*)d_in[1], (const u16*)d_in[2], (const u16*)d_in[4], (const u16*)d_in[5],
                               (char*)d_ws, (u16*)d_out);
}
```
